```python
import torch

def _softmax(x, axis=-1):
    return torch.softmax(x, dim=axis)

def _gelu(x):
    return torch.nn.functional.gelu(x, approximate='tanh')

def _silu(x):
    return torch.nn.functional.silu(x)

def _sigmoid(x):
    return torch.sigmoid(x)

def _layernorm(x, axis=-1, eps=1e-05):
    m = x.mean(dim=axis, keepdim=True)
    v = x.var(dim=axis, keepdim=True, unbiased=False)
    return (x - m) / torch.sqrt(v + eps)

def _scatter_add(tgt, idx, src, axis=0):
    return tgt.clone().index_add_(axis, idx, src)

def _segment_sum(src, seg, n):
    out = torch.zeros((n,) + src.shape[1:], dtype=src.dtype)
    return out.index_add_(0, seg, src)

def _pad(x, pw):
    flat = tuple((v for pair in reversed(pw) for v in pair))
    return torch.nn.functional.pad(x, flat)
kernel_name = 'adaLN_cond__memory_aug_recurrent_mem_tr__swiglu_ffn__prompt'

def setup_inputs():

    _w = lambda a: (lambda t: t.to(torch.bfloat16) if t.is_floating_point() else t)(torch.as_tensor(a))

    def _A_setup():
        B, S, Dm = (1, 2048, 576)
        H, Dh = (32, 128)
        M, seg = (8, 32)
        seg = min(seg, S)
        S = S // seg * seg
        return {'x': _w((lambda *s: torch.randn(*s, dtype=torch.bfloat16) * 0.05)(B, S, Dm)), 'mem0': _w((lambda *s: torch.randn(*s, dtype=torch.bfloat16) * 0.4)(B, M, Dm)), 'Wq': _w((lambda *s: torch.randn(*s, dtype=torch.bfloat16) * 3.2)(Dm, H, Dh) * Dm ** (-0.5)), 'Wk': _w((lambda *s: torch.randn(*s, dtype=torch.bfloat16) * 3.2)(Dm, H, Dh) * Dm ** (-0.5)), 'Wv': _w((lambda *s: torch.randn(*s, dtype=torch.bfloat16) * 0.4)(Dm, H, Dh) * Dm ** (-0.5)), 'Wo': _w((lambda *s: torch.randn(*s, dtype=torch.bfloat16) * 0.4)(H, Dh, Dm) * (H * Dh) ** (-0.5)), 'seg_len': seg}
    _A_in = _A_setup()

    def _B_setup():
        B, S, D, F = (1, 2048, 576, 4608)
        return {'x': _w((lambda *s: torch.randn(*s, dtype=torch.bfloat16) * 0.05)(B, S, D)), 'w_gate': _w((lambda *s: torch.randn(*s, dtype=torch.bfloat16) * 0.05)(D, F)), 'w_up': _w((lambda *s: torch.randn(*s, dtype=torch.bfloat16) * 0.05)(D, F)), 'w_down': _w((lambda *s: torch.randn(*s, dtype=torch.bfloat16) * 0.05)(F, D))}
    _B_in = _B_setup()
    return {'A_x': _A_in['x'], 'A_mem0': _A_in['mem0'], 'A_Wq': _A_in['Wq'], 'A_Wk': _A_in['Wk'], 'A_Wv': _A_in['Wv'], 'A_Wo': _A_in['Wo'], 'A_seg_len': _A_in['seg_len'], 'B_x': _B_in['x'], 'B_w_gate': _B_in['w_gate'], 'B_w_up': _B_in['w_up'], 'B_w_down': _B_in['w_down'], 'c': _w((lambda *s: torch.randn(*s, dtype=torch.bfloat16) * 0.1)(1, 576) * 8.0), 'w_mod': _w((lambda *s: torch.randn(*s, dtype=torch.bfloat16) * 0.1)(576, 6 * 576) * 576 ** (-0.5) * 8.0)}

def reference(A_x, A_mem0, A_Wq, A_Wk, A_Wv, A_Wo, A_seg_len, B_x, B_w_gate, B_w_up, B_w_down, c, w_mod):

    def _A_ref(x, mem0, Wq, Wk, Wv, Wo, seg_len):
        B, S, Dm = x.shape
        H, Dh = (Wq.shape[1], Wq.shape[2])
        M = mem0.shape[1]
        scale = Dh ** (-0.5)
        n_seg = S // seg_len
        L = M + seg_len
        idx = torch.arange(L)
        mask = (idx[:, None] < M) | (idx[:, None] >= idx[None, :])
        mem = mem0
        outs = []
        for s in range(n_seg):
            xs = x[:, s * seg_len:(s + 1) * seg_len, :].clone()
            h = torch.concatenate([mem, xs], axis=1)
            q = torch.einsum('btd,dhe->bhte', h, Wq)
            k = torch.einsum('btd,dhe->bhte', h, Wk)
            v = torch.einsum('btd,dhe->bhte', h, Wv)
            scores = torch.einsum('bhqe,bhke->bhqk', q, k) * scale
            scores = torch.where(mask[None, None], scores, -1000000000.0)
            p = _softmax(scores, axis=-1)
            o = torch.einsum('bhqk,bhke->bhqe', p, v)
            o = torch.einsum('bhte,hed->btd', o, Wo)
            mem = o[:, :M, :].clone()
            outs.append(o[:, M:, :])
        return torch.concatenate(outs, axis=1)

    def _B_ref(x, w_gate, w_up, w_down):
        g = _silu(x @ w_gate)
        u = x @ w_up
        return g * u @ w_down
    x = A_x
    mod = (_silu(c) @ w_mod).reshape(c.shape[0], 6, 1, -1)
    sa, ca, ga, sb, cb, gb = (mod[:, 0].clone(), mod[:, 1].clone(), mod[:, 2].clone(), mod[:, 3].clone(), mod[:, 4].clone(), mod[:, 5].clone())
    h = _layernorm(x, axis=-1) * (1 + ca) + sa
    x = x + ga * _A_ref(h, A_mem0, A_Wq, A_Wk, A_Wv, A_Wo, A_seg_len)
    h = _layernorm(x, axis=-1) * (1 + cb) + sb
    x = x + gb * _B_ref(h, B_w_gate, B_w_up, B_w_down)
    return x
```

```cpp
#include <hip/hip_runtime.h>
#include <hip/hip_bf16.h>
#include <stdint.h>

#define DI __device__ __forceinline__
#define LAS __attribute__((address_space(3)))
typedef __bf16 bf16;
typedef __bf16 bf8 __attribute__((ext_vector_type(8)));
typedef __bf16 bf4 __attribute__((ext_vector_type(4)));
typedef __bf16 bf2 __attribute__((ext_vector_type(2)));
typedef __bf16 bf16v __attribute__((ext_vector_type(16)));
typedef float f4 __attribute__((ext_vector_type(4)));
typedef float f2 __attribute__((ext_vector_type(2)));
typedef unsigned u4 __attribute__((ext_vector_type(4)));
typedef unsigned u2 __attribute__((ext_vector_type(2)));
typedef short s4 __attribute__((ext_vector_type(4)));
typedef short s8 __attribute__((ext_vector_type(8)));
typedef __amdgpu_buffer_rsrc_t rsrc_t;

constexpr int D_ = 576, NH = 32, NSEG = 64, F_ = 4608;

constexpr unsigned OFF_TMO = 0;
constexpr unsigned OFF_FLAGS = 256;
enum { FL_H1 = 0, FL_K, FL_VW, FL_VWM, FL_P, FL_ATT, FL_H2, FL_ACT, FL_P2, NFLAG };
constexpr unsigned ZERO_BYTES = OFF_FLAGS + NFLAG * NSEG * 256;
constexpr unsigned OFF_MRING = ZERO_BYTES;
constexpr unsigned M_SLOT = 9216 + 128, M_NSLOT = 4;
constexpr unsigned OFF_PRING = OFF_MRING + M_NSLOT * M_SLOT;
constexpr unsigned P_SLOT = 20480, P_NSLOT = 3;
constexpr unsigned OFF_WRING = OFF_PRING + P_NSLOT * P_SLOT;
constexpr unsigned W_SLOT = 36 * 32 * 256, W_NSLOT = 3;
constexpr unsigned RING_END = OFF_WRING + W_NSLOT * W_SLOT;
constexpr unsigned OFF_MOD = (RING_END + 255) / 256 * 256;
constexpr unsigned OFF_H1 = OFF_MOD + 8192;
constexpr unsigned OFF_KF = OFF_H1 + 2048 * 1152;
constexpr unsigned OFF_VW = OFF_KF + 64 * 32 * 16384;
constexpr unsigned OFF_VWM = OFF_VW + 64u * 36 * 32 * 1024;
constexpr unsigned OFF_PR = OFF_VWM + 64u * 36 * 32 * 256;
constexpr unsigned OFF_PRM = OFF_PR + 64 * 32 * 2048;
constexpr unsigned OFF_ATT = OFF_PRM + 64 * 8 * 2048;
constexpr unsigned OFF_X1 = OFF_ATT + 64 * 36 * 1024;
constexpr unsigned OFF_H2 = OFF_X1 + 2048 * 1152;
constexpr unsigned OFF_ACT = OFF_H2 + 2048 * 1152;
constexpr unsigned OFF_P2 = OFF_ACT + 64 * 36 * 8192;
constexpr unsigned WS_END = OFF_P2 + 64 * 36 * 8192;
static_assert(WS_END <= (256u << 20), "workspace");
static_assert(OFF_MRING % 128 == 0 && OFF_PRING % 128 == 0 && OFF_WRING % 128 == 0, "align");

#define FLAG_SLEEP 10
#define T_PNOM 370
#define T_OP 205
#define T_OW (T_OP - 25)
#define T_WM 15
#define T_S 150
#define POST_LEVEL 9
constexpr unsigned SENT = 0xFFFFFFFFu;
constexpr unsigned SPIN_MAX = 1u << 20;
constexpr int LDS_BYTES = 160 * 1024;

struct Params {
    const bf16* x; const bf16* mem0; const bf16* Wq; const bf16* Wk; const bf16* Wv; const bf16* Wo;
    const bf16* Wg; const bf16* Wu; const bf16* Wd; bf16* out; char* ws;
};

DI float bf2f(bf16 v) { return (float)v; }
DI float rbf(float v) { return (float)(bf16)v; }
DI float lo16(unsigned w) { return __uint_as_float(w << 16); }
DI float hi16(unsigned w) { return __uint_as_float(w & 0xffff0000u); }
DI unsigned pk2(float a, float b) { bf2 r; r[0] = (bf16)a; r[1] = (bf16)b; return __builtin_bit_cast(unsigned, r); }
DI u4 ld16(rsrc_t r, unsigned voff, unsigned soff) { return __builtin_amdgcn_raw_buffer_load_b128(r, voff, soff, 16); }
DI u2 ld8(rsrc_t r, unsigned voff, unsigned soff) { return __builtin_amdgcn_raw_buffer_load_b64(r, voff, soff, 16); }
DI unsigned ld4(rsrc_t r, unsigned voff, unsigned soff) { return __builtin_amdgcn_raw_buffer_load_b32(r, voff, soff, 16); }
DI void st16(rsrc_t r, unsigned voff, unsigned soff, u4 v) { __builtin_amdgcn_raw_buffer_store_b128(v, r, voff, soff, 16); }
DI void st8(rsrc_t r, unsigned voff, unsigned soff, u2 v) { __builtin_amdgcn_raw_buffer_store_b64(v, r, voff, soff, 16); }
DI void st4(rsrc_t r, unsigned voff, unsigned soff, unsigned v) { __builtin_amdgcn_raw_buffer_store_b32(v, r, voff, soff, 16); }
DI void st2(rsrc_t r, unsigned voff, unsigned soff, unsigned short v) { __builtin_amdgcn_raw_buffer_store_b16(v, r, voff, soff, 16); }
#define CFENCE() asm volatile("" ::: "memory")
#define DRAIN() asm volatile("s_waitcnt vmcnt(0)" ::: "memory")
#define BAR() asm volatile("s_waitcnt lgkmcnt(0)\n\ts_barrier" ::: "memory")
DI s4 trd(LAS char* p) { return __builtin_amdgcn_ds_read_tr16_b64_v4i16((LAS s4*)p); }
DI bf8 cat8(s4 a, s4 b) { return __builtin_bit_cast(bf8, __builtin_shufflevector(a, b, 0, 1, 2, 3, 4, 5, 6, 7)); }
DI f4 mfma(bf8 a, bf8 b, f4 c) { return __builtin_amdgcn_mfma_f32_16x16x32_bf16(a, b, c, 0, 0, 0); }
DI f4 smf(bf8 a, bf16v b, f4 c, int idx) { return __builtin_amdgcn_smfmac_f32_16x16x64_bf16(a, b, c, idx, 0, 0); }
DI bf8 asbf8(u4 v) { return __builtin_bit_cast(bf8, v); }

struct Ctx { char* ws; rsrc_t r; int tid, lane, wave; LAS char* lds; };
#define CNT(k, v) do {} while (0)
#define STMP(s, k) do {} while (0)
#define STMPV(s, k, v) do {} while (0)
#define STMPW(wv, s, k) do {} while (0)
#define STMP_DUMP() do {} while (0)
#define STMPMAX(s, k) do {} while (0)

DI bool spin_fail(const Ctx& c, unsigned& spins) {
    ++spins;
    if ((spins & 1023u) == 0u) {
        if (spins >= SPIN_MAX) { if (c.lane == 0) st4(c.r, OFF_TMO, 0, 1u); return true; }
        CFENCE();
        if (__builtin_amdgcn_readfirstlane(ld4(c.r, OFF_TMO, 0)) != 0) return true;
    }
    return false;
}
DI void wait_flags(const Ctx& c, int type, int s, int w0, int n) {
    const unsigned base = OFF_FLAGS + (type * NSEG + s) * 256 + w0 * 4;
    unsigned spins = 0;
    for (;;) {
        CFENCE();
        unsigned v = ld4(c.r, (c.lane < n ? c.lane : 0) * 4, base);
        if (__all(v != 0u)) break;
        __builtin_amdgcn_s_sleep(FLAG_SLEEP);
        if (spin_fail(c, spins)) break;
    }
    CFENCE();
}
DI bool check_flags(const Ctx& c, int type, int s, int w0, int n) {
    CFENCE();
    unsigned v = ld4(c.r, (c.lane < n ? c.lane : 0) * 4, OFF_FLAGS + (type * NSEG + s) * 256 + w0 * 4);
    return __all(v != 0u);
}
DI void wait_mask(const Ctx& c, int type, int w, int s, unsigned long long& mask) {
    unsigned spins = 0;
    while (!((mask >> s) & 1ull)) {
        CFENCE();
        unsigned v = ld4(c.r, c.lane * 256, OFF_FLAGS + type * NSEG * 256 + w * 4);
        mask = __ballot(v != 0u);
        if ((mask >> s) & 1ull) break;
        __builtin_amdgcn_s_sleep(FLAG_SLEEP);
        if (spin_fail(c, spins)) break;
    }
    CFENCE();
}
DI void wait_mask4(const Ctx& c, int type, int w, int s, unsigned long long& mask) {
    unsigned spins = 0;
    while (!((mask >> s) & 1ull)) {
        CFENCE();
        u4 v = ld16(c.r, c.lane * 256, OFF_FLAGS + type * NSEG * 256 + w * 4);
        mask = __ballot(v.x != 0u && v.y != 0u && v.z != 0u && v.w != 0u);
        if ((mask >> s) & 1ull) break;
        __builtin_amdgcn_s_sleep(FLAG_SLEEP);
        if (spin_fail(c, spins)) break;
    }
    CFENCE();
}
DI void set_flag(const Ctx& c, int type, int s, int w) { st4(c.r, 0, OFF_FLAGS + (type * NSEG + s) * 256 + w * 4, 1u); }
DI void publish(const Ctx& c, int type, int s, int w) { DRAIN(); BAR(); if (c.tid == 0) set_flag(c, type, s, w); }

template <int NC, int NROWS>
DI void stage_panel(const bf16* W, int ldw, LAS char* lds, int tid) {
    constexpr int CPR = NC / 8, TOT = NROWS * CPR, IT = (TOT + 511) / 512, RS = NC * 2 + 16;
    u4 v[IT];
#pragma unroll
    for (int i = 0; i < IT; ++i) { int n = tid + i * 512; if (TOT % 512 == 0 || n < TOT) { int r = n / CPR, ch = n % CPR; v[i] = *(const u4*)(W + (size_t)r * ldw + ch * 8); } }
#pragma unroll
    for (int i = 0; i < IT; ++i) { int n = tid + i * 512; if (TOT % 512 == 0 || n < TOT) { int r = n / CPR, ch = n % CPR; *(LAS u4*)(lds + r * RS + ch * 16) = v[i]; } }
}
DI bf8 frag_dense(LAS char* lds, int RS, int row0, int cb, int lane) {
    const int g = lane >> 4, q = (lane & 15) >> 2, p = lane & 3;
    LAS char* a = lds + (row0 + 8 * g + q) * RS + cb + p * 8;
    return cat8(trd(a), trd(a + 4 * RS));
}
DI bf8 frag_perm(LAS char* lds, int RS, int row0, int cb, int lane) {
    const int g = lane >> 4, q = (lane & 15) >> 2, p = lane & 3;
    LAS char* a = lds + (row0 + 4 * g + q) * RS + cb + p * 8;
    return cat8(trd(a), trd(a + 16 * RS));
}
DI bf16v frag_sparse(LAS char* lds, int RS, int row0, int cb, int lane) {
    const int gb = lane >> 4, q = (lane & 15) >> 2, p = lane & 3;
    LAS char* a = lds + (row0 + 16 * (gb >> 1) + 8 * (q >> 1) + 4 * (gb & 1) + (q & 1)) * RS + cb + p * 8;
    s4 e0 = trd(a), e1 = trd(a + 2 * RS), e2 = trd(a + 32 * RS), e3 = trd(a + 34 * RS);
    s8 lo = __builtin_shufflevector(e0, e1, 0, 1, 2, 3, 4, 5, 6, 7), hi = __builtin_shufflevector(e2, e3, 0, 1, 2, 3, 4, 5, 6, 7);
    return __builtin_bit_cast(bf16v, __builtin_shufflevector(lo, hi, 0, 1, 2, 3, 4, 5, 6, 7, 8, 9, 10, 11, 12, 13, 14, 15));
}
DI unsigned tsw(unsigned a) { return a ^ ((a >> 2) & 64u); }
DI bf8 a_sparse(LAS char* T, int k0, int lane) {
    const int ga = lane >> 4, q = (lane & 15) >> 2, p = lane & 3;
    const unsigned a = (unsigned)(k0 + 16 * ga + 8 * (p >> 1) + q) * 16u;
    return cat8(trd(T + tsw(a) + (p & 1) * 8), trd(T + tsw(a + 64u) + (p & 1) * 8));
}
DI bf8 t_perm(LAS char* T, int k0, int lane) {
    const int g = lane >> 4, q = (lane & 15) >> 2, p = lane & 3;
    const unsigned a = (unsigned)(k0 + 4 * g + q) * 16u;
    return cat8(trd(T + tsw(a) + (p & 1) * 8), trd(T + tsw(a + 256u) + (p & 1) * 8));
}
DI float halves(float a, float b) {
    auto r = __builtin_amdgcn_permlane32_swap(__float_as_uint(a), __float_as_uint(b), false, false);
    return __uint_as_float(r[0]) + __uint_as_float(r[1]);
}
DI float xmax16(float v) { auto r = __builtin_amdgcn_permlane16_swap(__float_as_uint(v), __float_as_uint(v), false, false); return fmaxf(__uint_as_float(r[0]), __uint_as_float(r[1])); }
DI float xmax32(float v) { auto r = __builtin_amdgcn_permlane32_swap(__float_as_uint(v), __float_as_uint(v), false, false); return fmaxf(__uint_as_float(r[0]), __uint_as_float(r[1])); }
DI float xsum16(float v) { auto r = __builtin_amdgcn_permlane16_swap(__float_as_uint(v), __float_as_uint(v), false, false); return __uint_as_float(r[0]) + __uint_as_float(r[1]); }
DI float xsum32(float v) { auto r = __builtin_amdgcn_permlane32_swap(__float_as_uint(v), __float_as_uint(v), false, false); return __uint_as_float(r[0]) + __uint_as_float(r[1]); }
DI float xor8(float v) { return __uint_as_float(__builtin_amdgcn_update_dpp(0, __float_as_uint(v), 0x128  , 0xf, 0xf, false)); }
constexpr float SCALE = 0.08838834764831845f;
constexpr float LOG2E = 1.4426950408889634f;

DI unsigned now_t() { return (unsigned)__builtin_amdgcn_s_memrealtime(); }
DI void wait_until(unsigned t) {
    int d = (int)(t - now_t());
    if (d > 4000) return;
    while (d > 9) { __builtin_amdgcn_s_sleep(2); d = (int)(t - now_t()); }
    while (d > 0) d = (int)(t - now_t());
}
template <bool DATA>
DI unsigned gather_M(const Ctx& c, unsigned off, LAS char* T, unsigned& fails) {
    u4 a = {0, 0, 0, 0}, b; unsigned spins = 0;
    const unsigned so = off + c.wave * 1152;
    const unsigned vb = (c.lane & 8) ? 9216u - c.wave * 1152 + (c.lane & 7) * 16 : (DATA ? 1024u + (c.lane & 7) * 16 : 9216u - c.wave * 1152 + (c.lane & 7) * 16);
    for (;;) {
        CFENCE();
        if (DATA) a = ld16(c.r, c.lane * 16, so);
        b = ld16(c.r, vb, so);
        unsigned mx = max(max(max(a.x, a.y), max(a.z, a.w)), max(max(b.x, b.y), max(b.z, b.w)));
        if (!__any(mx == SENT)) break;
        ++fails;
        if (spin_fail(c, spins)) break;
    }
    if (DATA) {
        *(LAS u4*)(T + tsw(c.wave * 1152 + c.lane * 16)) = a;
        if (c.lane < 8) *(LAS u4*)(T + tsw(c.wave * 1152 + 1024 + c.lane * 16)) = b;
    }
    return __builtin_amdgcn_readlane(b.x, 8);
}
DI void load_mem0(const bf16* mem0, LAS char* T, int tid) {
    for (int n = tid; n < 8 * 576; n += 512) { int r = n / 576, k = n % 576; *(LAS bf16*)(T + tsw(k * 16) + r * 2) = mem0[n]; }
}

DI void role_sc(const Params& p, const Ctx& c, LAS char* lds, int h) {
    const int lane = c.lane, w = c.wave;
    bf16v wq[9], wk[9];
    stage_panel<128, 576>(p.Wq + h * 128, 4096, lds, c.tid); __syncthreads();
#pragma unroll
    for (int kb = 0; kb < 9; ++kb) wq[kb] = frag_sparse(lds, 272, kb * 64, w * 32, lane);
    __syncthreads();
    stage_panel<128, 576>(p.Wk + h * 128, 4096, lds, c.tid); __syncthreads();
#pragma unroll
    for (int kb = 0; kb < 9; ++kb) wk[kb] = frag_sparse(lds, 272, kb * 64, w * 32, lane);
    __syncthreads();
    LAS char* T = lds;
    LAS char* TQ = lds + 9216;
    LAS char* TK = lds + 11264;
    LAS char* KS = lds + 13312;
    load_mem0(p.mem0, T, c.tid);
    wait_flags(c, FL_K, 0, h, 1);
    *(LAS u4*)(KS + w * 1024 + lane * 16) = ld16(c.r, lane * 16, OFF_KF + h * 16384 + w * 1024);
    *(LAS u4*)(KS + 8192 + w * 1024 + lane * 16) = ld16(c.r, lane * 16, OFF_KF + h * 16384 + 8192 + w * 1024);
    BAR();
    const int idx = (lane & 8) ? 0xEEEE : 0x4444;
    unsigned long long kmask = 0; unsigned anchor = 0, fails = 0;
    wait_mask(c, FL_K, h, 1, kmask);
    for (int s = 0; s < NSEG; ++s) {
        STMPW(0, s, 0);
        f4 aq = {0, 0, 0, 0}, ak = {0, 0, 0, 0};
#pragma unroll
        for (int kb = 0; kb < 9; ++kb) { bf8 a = a_sparse(T, kb * 64, lane); aq = smf(a, wq[kb], aq, idx); ak = smf(a, wk[kb], ak, idx); }
        {
            float v0 = halves(aq[0], ak[0]), v1 = halves(aq[1], ak[1]), v2 = halves(aq[2], ak[2]), v3 = halves(aq[3], ak[3]);
            u2 o = {pk2(v0, v1), pk2(v2, v3)};
            const unsigned e = w * 16 + (lane & 15);
            *(LAS u2*)((lane < 32 ? TQ : TK) + tsw(e * 16) + ((lane >> 4) & 1) * 8) = o;
        }
        u4 kv, qv;
        BAR();
        STMPW(0, s, 1);
        if (s < NSEG - 1) { kv = ld16(c.r, lane * 16, OFF_KF + ((s + 1) * 32 + h) * 16384 + w * 1024); qv = ld16(c.r, lane * 16, OFF_KF + ((s + 1) * 32 + h) * 16384 + 8192 + w * 1024); }
        if (w == 0) {
            if (s < NSEG - 1) {
                f4 s0 = {0, 0, 0, 0}, s1 = {0, 0, 0, 0}, s2 = {0, 0, 0, 0};
#pragma unroll
                for (int ks = 0; ks < 4; ++ks) {
                    bf8 qf = t_perm(TQ, ks * 32, lane), kf = t_perm(TK, ks * 32, lane);
                    bf8 k0 = asbf8(*(LAS u4*)(KS + ks * 1024 + lane * 16)), k1 = asbf8(*(LAS u4*)(KS + 4096 + ks * 1024 + lane * 16));
                    s0 = mfma(k0, qf, s0); s1 = mfma(k1, qf, s1); s2 = mfma(kf, qf, s2);
                }
                const bool hf = lane & 8; const int g = lane >> 4;
                float v[6];
#pragma unroll
                for (int r = 0; r < 4; ++r) v[r] = hf ? s1[r] : s0[r];
                v[4] = hf ? s2[2] : s2[0]; v[5] = hf ? s2[3] : s2[1];
#pragma unroll
                for (int r = 0; r < 6; ++r) v[r] = rbf(rbf(v[r]) * SCALE);
                if (g >= 2) { v[4] = -INFINITY; v[5] = -INFINITY; }
                float m = fmaxf(fmaxf(fmaxf(v[0], v[1]), fmaxf(v[2], v[3])), fmaxf(v[4], v[5]));
                m = fmaxf(m, xor8(m)); m = xmax16(m); m = xmax32(m);
                float sum = 0.f;
#pragma unroll
                for (int r = 0; r < 6; ++r) { v[r] = __builtin_amdgcn_exp2f((v[r] - m) * LOG2E); sum += v[r]; }
                sum += xor8(sum); sum = xsum16(sum); sum = xsum32(sum);
                const float inv = __builtin_amdgcn_rcpf(sum);
                u2 ps = {pk2(v[0] * inv, v[1] * inv), pk2(v[2] * inv, v[3] * inv)};
                unsigned pm = pk2(v[4] * inv, v[5] * inv);
                const unsigned ox = __float_as_uint(xor8(__uint_as_float(ps.x))), oy = __float_as_uint(xor8(__uint_as_float(ps.y)));
                const unsigned pmo = __float_as_uint(xor8(__uint_as_float(pm)));
                const unsigned m01 = hf ? pmo : pm, m23 = hf ? pm : pmo;
                auto r01 = __builtin_amdgcn_permlane16_swap(m01, m01, false, false), r23 = __builtin_amdgcn_permlane16_swap(m23, m23, false, false);
                u4 o = hf ? (u4){r01[0], r23[0], r01[1], r23[1]} : (u4){ps.x, ps.y, ox, oy};
                o.x = min(o.x, SENT - 1); o.y = min(o.y, SENT - 1); o.z = min(o.z, SENT - 1); o.w = min(o.w, SENT - 1);
                const unsigned slot = OFF_PRING + (s % P_NSLOT) * P_SLOT;
                const unsigned vo = hf ? (g == 0 ? 512u + (lane & 7) * 16 : 0x80000000u) : (unsigned)(g * 8 + (lane & 7)) * 16;
                st16(c.r, vo, slot + h * 640, o);
                STMP(s, 2);
                if (s >= 1) st16(c.r, vo, OFF_PRING + ((s + P_NSLOT - 1) % P_NSLOT) * P_SLOT + h * 640, (u4){SENT, SENT, SENT, SENT});
            }
        } else if (w == 1 || w == 2) {
            const int qt = w - 1;
            f4 s0 = {0, 0, 0, 0}, s1 = {0, 0, 0, 0}, sm = {0, 0, 0, 0};
#pragma unroll
            for (int ks = 0; ks < 4; ++ks) {
                bf8 km = t_perm(TK, ks * 32, lane);
                bf8 qf = asbf8(*(LAS u4*)(KS + 8192 + qt * 4096 + ks * 1024 + lane * 16));
                bf8 k0 = asbf8(*(LAS u4*)(KS + ks * 1024 + lane * 16)), k1 = asbf8(*(LAS u4*)(KS + 4096 + ks * 1024 + lane * 16));
                s0 = mfma(k0, qf, s0); s1 = mfma(k1, qf, s1); sm = mfma(km, qf, sm);
            }
            const int g = lane >> 4, q = 16 * qt + (lane & 15);
            float v[12];
#pragma unroll
            for (int r = 0; r < 4; ++r) {
                v[r] = (4 * g + r <= q) ? rbf(rbf(s0[r]) * SCALE) : -INFINITY;
                v[4 + r] = (16 + 4 * g + r <= q) ? rbf(rbf(s1[r]) * SCALE) : -INFINITY;
                v[8 + r] = (g < 2) ? rbf(rbf(sm[r]) * SCALE) : -INFINITY;
            }
            float m = v[0];
#pragma unroll
            for (int r = 1; r < 12; ++r) m = fmaxf(m, v[r]);
            m = xmax16(m); m = xmax32(m);
            float sum = 0.f;
#pragma unroll
            for (int r = 0; r < 12; ++r) { v[r] = __builtin_amdgcn_exp2f((v[r] - m) * LOG2E); sum += v[r]; }
            sum = xsum16(sum); sum = xsum32(sum);
            const float inv = __builtin_amdgcn_rcpf(sum);
            u4 o = {pk2(v[0] * inv, v[1] * inv), pk2(v[2] * inv, v[3] * inv), pk2(v[4] * inv, v[5] * inv), pk2(v[6] * inv, v[7] * inv)};
            st16(c.r, lane * 16, OFF_PR + ((s * 32 + h) * 2 + qt) * 1024, o);
            const unsigned mx = pk2(v[8] * inv, v[9] * inv), my = pk2(v[10] * inv, v[11] * inv);
            auto rx = __builtin_amdgcn_permlane16_swap(mx, mx, false, false), ry = __builtin_amdgcn_permlane16_swap(my, my, false, false);
            st16(c.r, lane < 16 ? (unsigned)((h & 3) * 16 + lane) * 16 : 0x80000000u, OFF_PRM + ((s * 8 + (h >> 2)) * 2 + qt) * 1024, (u4){rx[0], ry[0], rx[1], ry[1]});
            if (s == NSEG - 1) { DRAIN(); if (lane == 0) set_flag(c, FL_P, s, h * 2 + qt); }
        }
        if (s == NSEG - 1) break;
        BAR();
        *(LAS u4*)(KS + w * 1024 + lane * 16) = kv; *(LAS u4*)(KS + 8192 + w * 1024 + lane * 16) = qv;
        if (s + 2 < NSEG) wait_mask(c, FL_K, h, s + 2, kmask);
        STMPW(0, s, 5);
        if (s >= 1) { if (w == 0) wait_until(anchor + T_PNOM); BAR(); }
        STMPW(0, s, 3);
        anchor = gather_M<true>(c, OFF_MRING + ((s + 1) % M_NSLOT) * M_SLOT, T, fails);
        if (w == 1 || w == 2) { DRAIN(); if (lane == 0) set_flag(c, FL_P, s, h * 2 + (w - 1)); }
        STMPMAX(s, 4);
        BAR();
    }
    CNT(0, fails);
}

DI void role_vc(const Params& p, const Ctx& c, LAS char* lds, int h) {
    const int lane = c.lane, w = c.wave;
    bf16v wv[9], wo[5][2];
    stage_panel<128, 576>(p.Wv + h * 128, 4096, lds, c.tid); __syncthreads();
#pragma unroll
    for (int kb = 0; kb < 9; ++kb) wv[kb] = frag_sparse(lds, 272, kb * 64, w * 32, lane);
    __syncthreads();
    stage_panel<576, 128>(p.Wo + (size_t)h * 128 * 576, 576, lds, c.tid); __syncthreads();
#pragma unroll
    for (int i = 0; i < 5; ++i) { const int ct = (w + 8 * i < 36) ? w + 8 * i : w;
        wo[i][0] = frag_sparse(lds, 1168, 0, ct * 32, lane); wo[i][1] = frag_sparse(lds, 1168, 64, ct * 32, lane); }
    __syncthreads();
    LAS char* T = lds;
    LAS char* TV = lds + 9216;
    load_mem0(p.mem0, T, c.tid);
    BAR();
    const int idx = (lane & 8) ? 0xEEEE : 0x4444;
    unsigned anchor = 0, fails = 0;
    const int col = lane & 15, R = lane >> 4;
    const int t1 = (R == 0) ? 0 : (R == 1) ? 2 : (R == 2) ? 1 : 3;
    const unsigned vo1 = (unsigned)(((w + 8 * t1) * 32 + h) * 256 + col * 16);
    const unsigned vo2 = (w < 4 && R == 0) ? (unsigned)(((w + 32) * 32 + h) * 256 + col * 16) : 0x80000000u;
    for (int s = 0; s < NSEG; ++s) {
        STMPW(0, s, 0);
        f4 av = {0, 0, 0, 0};
#pragma unroll
        for (int kb = 0; kb < 9; ++kb) av = smf(a_sparse(T, kb * 64, lane), wv[kb], av, idx);
        {
            float v0 = halves(av[0], av[0]), v1 = halves(av[1], av[1]), v2 = halves(av[2], av[2]), v3 = halves(av[3], av[3]);
            u2 o = {pk2(v0, v1), pk2(v2, v3)};
            if (lane < 32) *(LAS u2*)(TV + tsw((w * 16 + (lane & 15)) * 16) + (lane >> 4) * 8) = o;
        }
        BAR();
        bf8 a0 = a_sparse(TV, 0, lane), a1 = a_sparse(TV, 64, lane);
        f4 d[5];
#pragma unroll
        for (int i = 0; i < 5; ++i) { d[i] = (f4){0, 0, 0, 0}; d[i] = smf(a0, wo[i][0], d[i], idx); d[i] = smf(a1, wo[i][1], d[i], idx); }
        const unsigned wslot = OFF_WRING + (s % W_NSLOT) * W_SLOT, pers = OFF_VWM + s * W_SLOT;
        const unsigned wold = OFF_WRING + ((s + W_NSLOT - 1) % W_NSLOT) * W_SLOT;
        u2 oa, ob, oc;
        { float v0 = halves(d[0][0], d[1][0]), v1 = halves(d[0][1], d[1][1]), v2 = halves(d[0][2], d[1][2]), v3 = halves(d[0][3], d[1][3]); oa = (u2){pk2(v0, v1), pk2(v2, v3)}; }
        { float v0 = halves(d[2][0], d[3][0]), v1 = halves(d[2][1], d[3][1]), v2 = halves(d[2][2], d[3][2]), v3 = halves(d[2][3], d[3][3]); ob = (u2){pk2(v0, v1), pk2(v2, v3)}; }
        auto sx = __builtin_amdgcn_permlane16_swap(oa.x, ob.x, false, false), sy = __builtin_amdgcn_permlane16_swap(oa.y, ob.y, false, false);
        u4 o1 = {min(sx[0], SENT - 1), min(sy[0], SENT - 1), min(sx[1], SENT - 1), min(sy[1], SENT - 1)};
        if (s < NSEG - 1) st16(c.r, vo1, wslot, o1);
        { float v0 = halves(d[4][0], d[4][0]), v1 = halves(d[4][1], d[4][1]), v2 = halves(d[4][2], d[4][2]), v3 = halves(d[4][3], d[4][3]); oc = (u2){pk2(v0, v1), pk2(v2, v3)}; }
        auto tx = __builtin_amdgcn_permlane16_swap(oc.x, oc.x, false, false), ty = __builtin_amdgcn_permlane16_swap(oc.y, oc.y, false, false);
        u4 o2 = {min(tx[0], SENT - 1), min(ty[0], SENT - 1), min(tx[1], SENT - 1), min(ty[1], SENT - 1)};
        if (s < NSEG - 1 && w < 4) st16(c.r, vo2, wslot, o2);
        STMPW(0, s, 2);
        st16(c.r, vo1, pers, o1); if (w < 4) st16(c.r, vo2, pers, o2);
        if (s == NSEG - 1) { publish(c, FL_VWM, s, h); break; }
        if (s >= 1) { const u4 z = {SENT, SENT, SENT, SENT}; st16(c.r, vo1, wold, z); if (w < 4) st16(c.r, vo2, wold, z); }
        DRAIN(); BAR();
        if (c.tid == 0) set_flag(c, FL_VWM, s, h);
        if (s >= 1) { if (w == 0) wait_until(anchor + T_PNOM); BAR(); }
        STMPW(0, s, 5);
        anchor = gather_M<true>(c, OFF_MRING + ((s + 1) % M_NSLOT) * M_SLOT, T, fails);
        STMPMAX(s, 6);
        BAR();
    }
    CNT(0, fails);
}

constexpr int NRD = 18;
DI void role_rd(const Params& p, const Ctx& c, LAS char* lds, int id) {
    const int lane = c.lane, w = c.wave;
    LAS char* RED = lds;
    unsigned anchor = 0, fails = 0, failsP = 0, failsW = 0, late = 0;
    unsigned long long vmask = 0;
    u4 vw[2][4];
    wait_mask4(c, FL_VW, 4 * w, 0, vmask);
#pragma unroll
    for (int t = 0; t < 2; ++t)
#pragma unroll
        for (int i = 0; i < 4; ++i) vw[t][i] = ld16(c.r, lane * 16, OFF_VW + ((2 * id + t) * 32 + 4 * w + i) * 1024);
    const unsigned pvo = ((lane >> 3) & 1) * 640 + ((lane >> 4) * 8 + (lane & 7)) * 16;
    const unsigned mvo = (lane >> 4) * 640 + 512 + (lane & 7) * 16;
    for (int s = 0; s < NSEG - 1; ++s) {
        STMPW(0, s, 0);
        if (w == 0) STMPV(s, 1, anchor);
        u4 wm[2], pa, pb, pmm; unsigned spins = 0;
        const unsigned wsl = OFF_WRING + (s % W_NSLOT) * W_SLOT + 2 * id * 8192 + w * 1024;
        const unsigned psl = OFF_PRING + (s % P_NSLOT) * P_SLOT + 4 * w * 640;
        if (s >= 1) { if (w == 0) wait_until(anchor + T_OW); BAR(); }
        wm[0] = ld16(c.r, lane * 16, wsl); wm[1] = ld16(c.r, lane * 16, wsl + 8192);
        if (s >= 1) { if (w == 0) wait_until(anchor + T_OP); BAR(); }
        STMPW(0, s, 3);
        for (;;) {
            CFENCE();
            pa = ld16(c.r, pvo, psl);
            pb = ld16(c.r, pvo, psl + 1280);
            pmm = ld16(c.r, mvo, psl);
            unsigned mx = max(max(max(pa.x, pa.y), max(pa.z, pa.w)), max(max(max(pb.x, pb.y), max(pb.z, pb.w)), max(max(pmm.x, pmm.y), max(pmm.z, pmm.w))));
            if (!__any(mx == SENT)) break;
            ++failsP;
            if (spin_fail(c, spins)) break;
        }
        for (;;) {
            unsigned mx = max(max(max(wm[0].x, wm[0].y), max(wm[0].z, wm[0].w)), max(max(wm[1].x, wm[1].y), max(wm[1].z, wm[1].w)));
            if (!__any(mx == SENT)) break;
            ++failsW;
            if (spin_fail(c, spins)) break;
            CFENCE();
            wm[0] = ld16(c.r, lane * 16, wsl); wm[1] = ld16(c.r, lane * 16, wsl + 8192);
        }
        STMPMAX(s, 4);
#pragma unroll
        for (int t = 0; t < 2; ++t) {
            f4 lo = {0, 0, 0, 0}, hi = {0, 0, 0, 0};
            lo = mfma(asbf8(pa), asbf8(vw[t][0]), lo); hi = mfma(asbf8(pa), asbf8(vw[t][1]), hi);
            lo = mfma(asbf8(pb), asbf8(vw[t][2]), lo); hi = mfma(asbf8(pb), asbf8(vw[t][3]), hi);
            lo = mfma(asbf8(pmm), asbf8(wm[t]), lo);
            f4 r;
#pragma unroll
            for (int q = 0; q < 4; ++q) { auto x = __builtin_amdgcn_permlane32_swap(__float_as_uint(hi[q]), __float_as_uint(hi[q]), false, false); r[q] = lo[q] + __uint_as_float(x[1]); }
            if (lane < 32) *(LAS f4*)(RED + w * 1024 + t * 512 + lane * 16) = r;
        }
        BAR();
        if (w < 2) {
            f4 t = *(LAS f4*)(RED + w * 512 + (lane & 31) * 16);
#pragma unroll
            for (int j = 1; j < 8; ++j) t += *(LAS f4*)(RED + j * 1024 + w * 512 + (lane & 31) * 16);
            const unsigned ox = pk2(t[0], t[1]), oy = pk2(t[2], t[3]);
            auto sx = __builtin_amdgcn_permlane16_swap(ox, ox, false, false), sy = __builtin_amdgcn_permlane16_swap(oy, oy, false, false);
            u4 o = {min(sx[0], SENT - 1), min(sy[0], SENT - 1), min(sx[1], SENT - 1), min(sy[1], SENT - 1)};
            unsigned vo = lane < 16 ? (unsigned)((2 * id + w) * 16 + lane) * 16 : 0x80000000u;
            if (id == NRD - 1 && w == 1) {
                const unsigned tn = now_t();
                const bool ontime = s >= 1 && (int)(anchor + T_PNOM - tn) >= T_WM;
                const unsigned an = ontime ? anchor + T_PNOM : tn + T_S;
                if (!ontime) ++late;
                if (lane >= 16 && lane < 24) { o = (u4){an == SENT ? 0u : an, 1u, 2u, 3u}; vo = 9216 + (lane - 16) * 16; }
            }
            st16(c.r, vo, OFF_MRING + ((s + 1) % M_NSLOT) * M_SLOT, o);
            STMPW(0, s, 7);
            if (s >= 1) st16(c.r, vo, OFF_MRING + ((s + M_NSLOT - 1) % M_NSLOT) * M_SLOT, (u4){SENT, SENT, SENT, SENT});
        }
        if (s + 1 < NSEG - 1) {
            wait_mask4(c, FL_VW, 4 * w, s + 1, vmask);
#pragma unroll
            for (int t = 0; t < 2; ++t)
#pragma unroll
                for (int i = 0; i < 4; ++i) vw[t][i] = ld16(c.r, lane * 16, OFF_VW + (((s + 1) * 36 + 2 * id + t) * 32 + 4 * w + i) * 1024);
            if (s >= 1) { if (w == 0) wait_until(anchor + T_PNOM); BAR(); }
            STMPW(0, s, 5);
            anchor = gather_M<false>(c, OFF_MRING + ((s + 1) % M_NSLOT) * M_SLOT, lds, fails);
            STMPW(0, s, 6);
        }
        BAR();
    }
    CNT(0, fails); CNT(1, failsP); CNT(2, late); CNT(3, failsW);
}

struct Rows { u4 v[5]; };
DI void rows_load(const Ctx& c, unsigned off, Rows& R) {
#pragma unroll
    for (int m = 0; m < 5; ++m) { const int n = c.tid + m * 512; R.v[m] = ld16(c.r, (m < 4 || n < 2304) ? (unsigned)n * 16 : 0x80000000u, off); }
}
DI void rows_store(const Ctx& c, const Rows& R, LAS char* img) {
#pragma unroll
    for (int m = 0; m < 5; ++m) { const int n = c.tid + m * 512; if (m < 4 || n < 2304) { const int r = n / 72, q = n % 72; *(LAS u4*)(img + r * 1168 + q * 16) = R.v[m]; } }
}
constexpr int IMG_BYTES = 37376;
template <int FIN, int NIN, int FOUT, class Body>
DI void row_pipeline(const Ctx& c, LAS char* lds, unsigned in_off, int out_word, Body body) {
    LAS char* IMG = lds; LAS unsigned* CTL = (LAS unsigned*)(lds + 2 * IMG_BYTES);
    Rows R;
    if (c.wave == 0) wait_flags(c, FIN, 0, 0, NIN);
    BAR();
    rows_load(c, in_off, R); rows_store(c, R, IMG);
    if (c.wave == 0) { const bool r = check_flags(c, FIN, 1, 0, NIN); if (c.lane == 0) CTL[0] = r; }
    BAR();
    bool rdy = CTL[0] != 0; int pending = -1;
    for (int s = 0; s < NSEG; ++s) {
        STMPW(0, s, 0);
        const bool last = s == NSEG - 1;
        if (last) rdy = false;
        if (rdy) rows_load(c, in_off + (s + 1) * 32 * 1152, R);
        bool nr = false;
        if (rdy && s + 2 < NSEG && c.wave == 0) nr = check_flags(c, FIN, s + 2, 0, NIN);
        STMPW(0, s, 1);
        body(s, IMG + (s & 1) * IMG_BYTES);
        STMPW(0, s, 2);
        if (!rdy) {
            DRAIN(); BAR();
            if (c.tid == 0) { if (pending >= 0) set_flag(c, FOUT, pending, out_word); set_flag(c, FOUT, s, out_word); }
            pending = -1;
            STMPW(0, s, 3);
            if (last) break;
            if (c.wave == 0) wait_flags(c, FIN, s + 1, 0, NIN);
            BAR();
            rows_load(c, in_off + (s + 1) * 32 * 1152, R);
            if (s + 2 < NSEG && c.wave == 0) nr = check_flags(c, FIN, s + 2, 0, NIN);
        }
        rows_store(c, R, IMG + ((s + 1) & 1) * IMG_BYTES);
        if (c.tid == 0) CTL[(s + 1) & 1] = nr;
        BAR();
        if (rdy) { if (pending >= 0 && c.tid == 0) set_flag(c, FOUT, pending, out_word); pending = s; }
        rdy = CTL[(s + 1) & 1] != 0;
        STMPW(0, s, 4);
    }
}

DI void role_qk(const Params& p, const Ctx& c, LAS char* lds, int h) {
    const int lane = c.lane, w = c.wave;
    bf8 wf[2][18];
    stage_panel<128, 576>(p.Wk + h * 128, 4096, lds, c.tid); __syncthreads();
    if (w < 4) {
#pragma unroll
        for (int t = 0; t < 2; ++t)
#pragma unroll
            for (int ks = 0; ks < 18; ++ks) wf[t][ks] = frag_dense(lds, 272, ks * 32, (2 * w + t) * 32, lane);
    }
    __syncthreads();
    stage_panel<128, 576>(p.Wq + h * 128, 4096, lds, c.tid); __syncthreads();
    if (w >= 4) {
#pragma unroll
        for (int t = 0; t < 2; ++t)
#pragma unroll
            for (int ks = 0; ks < 18; ++ks) wf[t][ks] = frag_dense(lds, 272, ks * 32, (2 * (w - 4) + t) * 32, lane);
    }
    __syncthreads();
    row_pipeline<FL_H1, 1, FL_K>(c, lds, OFF_H1, h, [&](int s, LAS char* IMG) {
        f4 acc[2][2];
#pragma unroll
        for (int a = 0; a < 2; ++a) for (int t = 0; t < 2; ++t) acc[a][t] = (f4){0, 0, 0, 0};
#pragma unroll
        for (int ks = 0; ks < 18; ++ks) {
#pragma unroll
            for (int t = 0; t < 2; ++t) {
                bf8 b = asbf8(*(LAS u4*)(IMG + (16 * t + (lane & 15)) * 1168 + ks * 64 + (lane >> 4) * 16));
                acc[0][t] = mfma(wf[0][ks], b, acc[0][t]); acc[1][t] = mfma(wf[1][ks], b, acc[1][t]);
            }
        }
#pragma unroll
        for (int t = 0; t < 2; ++t) {
            u4 o = {pk2(acc[0][t][0], acc[0][t][1]), pk2(acc[0][t][2], acc[0][t][3]), pk2(acc[1][t][0], acc[1][t][1]), pk2(acc[1][t][2], acc[1][t][3])};
            st16(c.r, (w >> 2) * 8192 + t * 4096 + (w & 3) * 1024 + lane * 16, OFF_KF + (s * 32 + h) * 16384, o);
        }
    });
}

DI void role_vw(const Params& p, const Ctx& c, LAS char* lds, int h) {
    const int lane = c.lane, w = c.wave;
    bf8 wv[18], wo[5][4];
    stage_panel<128, 576>(p.Wv + h * 128, 4096, lds, c.tid); __syncthreads();
#pragma unroll
    for (int ks = 0; ks < 18; ++ks) wv[ks] = frag_dense(lds, 272, ks * 32, w * 32, lane);
    __syncthreads();
    stage_panel<576, 128>(p.Wo + (size_t)h * 128 * 576, 576, lds, c.tid); __syncthreads();
#pragma unroll
    for (int i = 0; i < 5; ++i) { const int ct = (w + 8 * i < 36) ? w + 8 * i : w;
#pragma unroll
        for (int ks = 0; ks < 4; ++ks) wo[i][ks] = frag_perm(lds, 1168, ks * 32, ct * 32, lane); }
    __syncthreads();
    LAS char* VI = lds + 2 * IMG_BYTES + 64;
    row_pipeline<FL_H1, 1, FL_VW>(c, lds, OFF_H1, h, [&](int s, LAS char* IMG) {
        f4 acc[2] = {{0, 0, 0, 0}, {0, 0, 0, 0}};
#pragma unroll
        for (int ks = 0; ks < 18; ++ks)
#pragma unroll
            for (int t = 0; t < 2; ++t) acc[t] = mfma(wv[ks], asbf8(*(LAS u4*)(IMG + (16 * t + (lane & 15)) * 1168 + ks * 64 + (lane >> 4) * 16)), acc[t]);
#pragma unroll
        for (int t = 0; t < 2; ++t) { u2 o = {pk2(acc[t][0], acc[t][1]), pk2(acc[t][2], acc[t][3])}; *(LAS u2*)(VI + t * 4096 + (w >> 1) * 1024 + lane * 16 + (w & 1) * 8) = o; }
        BAR();
        bf8 a[2][4];
#pragma unroll
        for (int t = 0; t < 2; ++t)
#pragma unroll
            for (int ks = 0; ks < 4; ++ks) a[t][ks] = asbf8(*(LAS u4*)(VI + t * 4096 + ks * 1024 + lane * 16));
#pragma unroll
        for (int i = 0; i < 5; ++i) {
            const int ct = w + 8 * i;
            if (ct < 36) {
                f4 d0 = {0, 0, 0, 0}, d1 = {0, 0, 0, 0};
#pragma unroll
                for (int ks = 0; ks < 4; ++ks) { d0 = mfma(a[0][ks], wo[i][ks], d0); d1 = mfma(a[1][ks], wo[i][ks], d1); }
                u4 o = {pk2(d0[0], d0[1]), pk2(d0[2], d0[3]), pk2(d1[0], d1[1]), pk2(d1[2], d1[3])};
                st16(c.r, lane * 16, OFF_VW + ((s * 36 + ct) * 32 + h) * 1024, o);
            }
        }
    });
}

DI void role_pvw(const Params& p, const Ctx& c, LAS char* lds, int id) {
    const int lane = c.lane, w = c.wave;
    LAS char* RED = lds;
    u4 vw[2][4]; unsigned long long vmask = 0;
    wait_mask4(c, FL_VW, 4 * w, 0, vmask);
#pragma unroll
    for (int tl = 0; tl < 2; ++tl)
#pragma unroll
        for (int i = 0; i < 4; ++i) vw[tl][i] = ld16(c.r, lane * 16, OFF_VW + ((2 * id + tl) * 32 + 4 * w + i) * 1024);
    for (int s = 0; s < NSEG; ++s) {
        STMPW(0, s, 0);
        if (w == 0) { wait_flags(c, FL_P, s, 0, 64); STMP(s, 1); wait_flags(c, FL_VWM, s, 0, 32); }
        BAR();
        STMPW(0, s, 2);
        u4 pr[4][2], pm[2], wm[2];
#pragma unroll
        for (int i = 0; i < 4; ++i) for (int t = 0; t < 2; ++t) pr[i][t] = ld16(c.r, lane * 16, OFF_PR + ((s * 32 + 4 * w + i) * 2 + t) * 1024);
#pragma unroll
        for (int t = 0; t < 2; ++t) { pm[t] = ld16(c.r, lane * 16, OFF_PRM + ((s * 8 + w) * 2 + t) * 1024); wm[t] = ld16(c.r, lane * 16, OFF_VWM + s * W_SLOT + (2 * id + t) * 8192 + w * 1024); }
        LAS char* R = RED + (s & 1) * 32768;
#pragma unroll
        for (int tl = 0; tl < 2; ++tl)
#pragma unroll
            for (int t = 0; t < 2; ++t) {
                f4 d = {0, 0, 0, 0};
#pragma unroll
                for (int i = 0; i < 4; ++i) d = mfma(asbf8(pr[i][t]), asbf8(vw[tl][i]), d);
                d = mfma(asbf8(pm[t]), asbf8(wm[tl]), d);
                *(LAS f4*)(R + w * 4096 + tl * 2048 + t * 1024 + lane * 16) = d;
            }
        if (s + 1 < NSEG) {
            wait_mask4(c, FL_VW, 4 * w, s + 1, vmask);
#pragma unroll
            for (int tl = 0; tl < 2; ++tl)
#pragma unroll
                for (int i = 0; i < 4; ++i) vw[tl][i] = ld16(c.r, lane * 16, OFF_VW + (((s + 1) * 36 + 2 * id + tl) * 32 + 4 * w + i) * 1024);
        }
        BAR();
        {
            const int row = c.tid >> 4, col = c.tid & 15;
            const int o = (row >> 4) * 1024 + ((((row & 15) >> 2) * 16 + col) * 16) + (row & 3) * 4;
#pragma unroll
            for (int tl = 0; tl < 2; ++tl) {
                float t = 0.f;
#pragma unroll
                for (int j = 0; j < 8; ++j) t += *(LAS float*)(R + j * 4096 + tl * 2048 + o);
                bf16 r = (bf16)t;
                st2(c.r, c.tid * 2, OFF_ATT + (s * 36 + 2 * id + tl) * 1024, __builtin_bit_cast(unsigned short, r));
            }
        }
        STMPW(0, s, 3);
        publish(c, FL_ATT, s, id);
        STMPW(0, s, 4);
    }
}

DI float wave_sum(float v) {
    v += __uint_as_float(__builtin_amdgcn_update_dpp(0, __float_as_uint(v), 0x121, 0xf, 0xf, false));
    v += __uint_as_float(__builtin_amdgcn_update_dpp(0, __float_as_uint(v), 0x122, 0xf, 0xf, false));
    v += __uint_as_float(__builtin_amdgcn_update_dpp(0, __float_as_uint(v), 0x124, 0xf, 0xf, false));
    v += xor8(v);
    v = xsum16(v); v = xsum32(v);
    return v;
}
DI void row_load(const bf16* rowp, int lane, float (&x)[9]) {
#pragma unroll
    for (int m = 0; m < 4; ++m) { unsigned v = *(const unsigned*)(rowp + 128 * m + 2 * lane); x[2 * m] = lo16(v); x[2 * m + 1] = hi16(v); }
    x[8] = (float)rowp[512 + lane];
}
DI void row_store(const Ctx& c, unsigned off, const float (&y)[9]) {
#pragma unroll
    for (int m = 0; m < 4; ++m) st4(c.r, (128 * m + 2 * c.lane) * 2, off, pk2(y[2 * m], y[2 * m + 1]));
    bf16 r = (bf16)y[8]; st2(c.r, (512 + c.lane) * 2, off, __builtin_bit_cast(unsigned short, r));
}
DI void ln_mod(float (&x)[9], const float (&cc)[9], const float (&ss)[9]) {
    float t = 0.f;
#pragma unroll
    for (int j = 0; j < 9; ++j) t += x[j];
    const float mean = wave_sum(t) * (1.0f / 576.0f);
    t = 0.f;
#pragma unroll
    for (int j = 0; j < 9; ++j) { const float d = x[j] - mean; t += d * d; }
    const float var = wave_sum(t) * (1.0f / 576.0f);
    const float mb = rbf(mean), den = rbf(sqrtf(rbf(rbf(var) + 1e-5f)));
#pragma unroll
    for (int j = 0; j < 9; ++j) x[j] = rbf(rbf(rbf(rbf(x[j] - mb) / den) * cc[j]) + ss[j]);
}
DI void role_ln(const Params& p, const Ctx& c, LAS char* lds, int id) {
    const int lane = c.lane, w = c.wave;
    const bf16* mod = (const bf16*)(c.ws + OFF_MOD);
    {
        float sa[9], ca[9];
        row_load(mod, lane, sa); row_load(mod + 576, lane, ca);
        for (int s = id; s < NSEG; s += 16) {
            float x[4][9];
#pragma unroll
            for (int i = 0; i < 4; ++i) row_load(p.x + (size_t)(s * 32 + w * 4 + i) * 576, lane, x[i]);
#pragma unroll
            for (int i = 0; i < 4; ++i) { ln_mod(x[i], ca, sa); row_store(c, OFF_H1 + (s * 32 + w * 4 + i) * 1152, x[i]); }
            publish(c, FL_H1, s, 0);
            STMPW(0, s, 3);
        }
    }
    if (POST_LEVEL < 2) return;
    if (w < 2) {
        float ga[9], sb[9], cb[9];
        row_load(mod + 1152, lane, ga); row_load(mod + 1728, lane, sb); row_load(mod + 2304, lane, cb);
        const int rl = 2 * id + w;
        for (int s = 0; s < NSEG; ++s) {
            const int row = s * 32 + rl;
            float x[9], a[9];
            row_load(p.x + (size_t)row * 576, lane, x);
            STMPW(0, s, 0);
            wait_flags(c, FL_ATT, s, 0, 18);
            STMPW(0, s, 1);
#pragma unroll
            for (int m = 0; m < 4; ++m) { const int col = 128 * m + 2 * lane; unsigned v = ld4(c.r, ((col >> 4) * 32 + rl) * 32 + (col & 15) * 2, OFF_ATT + s * 36 * 1024); a[2 * m] = lo16(v); a[2 * m + 1] = hi16(v); }
            { const int col = 512 + lane; a[8] = __uint_as_float((unsigned)__builtin_amdgcn_raw_buffer_load_b16(c.r, ((col >> 4) * 32 + rl) * 32 + (col & 15) * 2, OFF_ATT + s * 36 * 1024, 16) << 16); }
#pragma unroll
            for (int j = 0; j < 9; ++j) x[j] = rbf(x[j] + rbf(ga[j] * a[j]));
            row_store(c, OFF_X1 + row * 1152, x);
            ln_mod(x, cb, sb);
            row_store(c, OFF_H2 + row * 1152, x);
            DRAIN();
            if (lane == 0) set_flag(c, FL_H2, s, rl);
            STMPW(0, s, 2);
        }
    } else if (w < 4 && POST_LEVEL >= 5) {
        const int rl = 2 * id + (w - 2), i = lane & 15;
        float gb[9][4];
#pragma unroll
        for (int nb = 0; nb < 9; ++nb) { u2 v = *(const u2*)(mod + 2880 + nb * 64 + 4 * i); gb[nb][0] = lo16(v.x); gb[nb][1] = hi16(v.x); gb[nb][2] = lo16(v.y); gb[nb][3] = hi16(v.y); }
        for (int s = 0; s < NSEG; ++s) {
            const int row = s * 32 + rl;
            wait_flags(c, FL_P2, s, 0, 36);
            STMPW(2, s, 4);
            u4 v[9]; u2 xv[9];
#pragma unroll
            for (int nb = 0; nb < 9; ++nb) { v[nb] = ld16(c.r, (lane >> 4) * 8192 + rl * 256 + i * 16, OFF_P2 + (s * 9 + nb) * 4 * 8192); xv[nb] = ld8(c.r, (nb * 64 + 4 * i) * 2, OFF_X1 + row * 1152); }
#pragma unroll
            for (int nb = 0; nb < 9; ++nb) {
                float t[4] = {__uint_as_float(v[nb].x), __uint_as_float(v[nb].y), __uint_as_float(v[nb].z), __uint_as_float(v[nb].w)};
#pragma unroll
                for (int j = 0; j < 4; ++j) t[j] = xsum32(xsum16(t[j]));
                const float x1[4] = {lo16(xv[nb].x), hi16(xv[nb].x), lo16(xv[nb].y), hi16(xv[nb].y)};
                u2 o = {pk2(x1[0] + rbf(gb[nb][0] * rbf(t[0])), x1[1] + rbf(gb[nb][1] * rbf(t[1]))), pk2(x1[2] + rbf(gb[nb][2] * rbf(t[2])), x1[3] + rbf(gb[nb][3] * rbf(t[3])))};
                if (lane < 16) *(u2*)(p.out + (size_t)row * 576 + nb * 64 + 4 * i) = o;
            }
            STMPW(2, s, 5);
        }
    }
}

DI void role_ffn1(const Params& p, const Ctx& c, LAS char* lds, int j) {
    const int lane = c.lane, w = c.wave;
    bf8 wg[18], wu[18];
    stage_panel<128, 576>(p.Wg + j * 128, F_, lds, c.tid); __syncthreads();
#pragma unroll
    for (int ks = 0; ks < 18; ++ks) wg[ks] = frag_dense(lds, 272, ks * 32, w * 32, lane);
    __syncthreads();
    stage_panel<128, 576>(p.Wu + j * 128, F_, lds, c.tid); __syncthreads();
#pragma unroll
    for (int ks = 0; ks < 18; ++ks) wu[ks] = frag_dense(lds, 272, ks * 32, w * 32, lane);
    __syncthreads();
    LAS char* AI = lds + 2 * IMG_BYTES + 64;
    row_pipeline<FL_H2, 32, FL_ACT>(c, lds, OFF_H2, j, [&](int s, LAS char* IMG) {
        f4 g[2] = {{0, 0, 0, 0}, {0, 0, 0, 0}}, u[2] = {{0, 0, 0, 0}, {0, 0, 0, 0}};
#pragma unroll
        for (int ks = 0; ks < 18; ++ks)
#pragma unroll
            for (int t = 0; t < 2; ++t) {
                bf8 b = asbf8(*(LAS u4*)(IMG + (16 * t + (lane & 15)) * 1168 + ks * 64 + (lane >> 4) * 16));
                g[t] = mfma(wg[ks], b, g[t]); u[t] = mfma(wu[ks], b, u[t]);
            }
#pragma unroll
        for (int t = 0; t < 2; ++t) {
            float a[4];
#pragma unroll
            for (int r = 0; r < 4; ++r) { const float gg = rbf(g[t][r]); const float sg = rbf(gg * __builtin_amdgcn_rcpf(1.0f + __builtin_amdgcn_exp2f(-gg * LOG2E))); a[r] = sg * rbf(u[t][r]); }
            *(LAS u2*)(AI + t * 4096 + (w >> 1) * 1024 + lane * 16 + (w & 1) * 8) = (u2){pk2(a[0], a[1]), pk2(a[2], a[3])};
        }
        BAR();
        st16(c.r, c.tid * 16, OFF_ACT + (s * 36 + j) * 8192, *(LAS u4*)(AI + c.tid * 16));
    });
}

DI void role_ffn2(const Params& p, const Ctx& c, LAS char* lds, int id) {
    const int lane = c.lane, w = c.wave;
    const int kq = id / 9, nb = id % 9, ct = w & 3, kh = w >> 2;
    bf8 wd[18];
#pragma unroll
    for (int ps = 0; ps < 2; ++ps) {
        stage_panel<64, 576>(p.Wd + (size_t)(kq * 1152 + ps * 576) * 576 + nb * 64, 576, lds, c.tid); __syncthreads();
        if (kh == ps) {
#pragma unroll
            for (int ks = 0; ks < 18; ++ks) wd[ks] = frag_perm(lds, 144, ks * 32, ct * 32, lane);
        }
        __syncthreads();
    }
    LAS char* AI = lds;
    LAS char* RED = lds + 73728;
    LAS unsigned* CTL = (LAS unsigned*)(lds + 73728 + 2 * 8704);
    u4 v[9];
    auto load = [&](int s) {
#pragma unroll
        for (int m = 0; m < 9; ++m) v[m] = ld16(c.r, c.tid * 16, OFF_ACT + (s * 36 + kq * 9 + m) * 8192); };
    auto store = [&](int s) {
#pragma unroll
        for (int m = 0; m < 9; ++m) *(LAS u4*)(AI + m * 8192 + c.tid * 16) = v[m]; };
    if (w == 0) wait_flags(c, FL_ACT, 0, kq * 9, 9);
    BAR();
    load(0); store(0);
    if (w == 0) { const bool r = check_flags(c, FL_ACT, 1, kq * 9, 9); if (lane == 0) CTL[0] = r; }
    BAR();
    bool rdy = CTL[0] != 0; int pending = -1;
    const int word = nb * 4 + kq;
    for (int s = 0; s < NSEG; ++s) {
        STMPW(0, s, 0);
        const bool last = s == NSEG - 1;
        if (last) rdy = false;
        if (rdy) load(s + 1);
        bool nr = false;
        if (rdy && s + 2 < NSEG && w == 0) nr = check_flags(c, FL_ACT, s + 2, kq * 9, 9);
        f4 d[2] = {{0, 0, 0, 0}, {0, 0, 0, 0}};
        LAS char* A = AI;
#pragma unroll
        for (int ks = 0; ks < 18; ++ks) {
            const int kk = kh * 18 + ks;
#pragma unroll
            for (int t = 0; t < 2; ++t) d[t] = mfma(wd[ks], asbf8(*(LAS u4*)(A + (kk >> 2) * 8192 + t * 4096 + (kk & 3) * 1024 + lane * 16)), d[t]);
        }
#pragma unroll
        for (int t = 0; t < 2; ++t) *(LAS f4*)(RED + kh * 8704 + (16 * t + (lane & 15)) * 272 + ct * 64 + (lane >> 4) * 16) = d[t];
        BAR();
        {
            const int row = c.tid >> 4, q = c.tid & 15;
            f4 r = *(LAS f4*)(RED + row * 272 + q * 16) + *(LAS f4*)(RED + 8704 + row * 272 + q * 16);
            st16(c.r, c.tid * 16, OFF_P2 + ((s * 9 + nb) * 4 + kq) * 8192, __builtin_bit_cast(u4, r));
        }
        STMPW(0, s, 2);
        if (!rdy) {
            DRAIN(); BAR();
            if (c.tid == 0) { if (pending >= 0) set_flag(c, FL_P2, pending, word); set_flag(c, FL_P2, s, word); }
            pending = -1;
            STMPW(0, s, 3);
            if (last) break;
            if (w == 0) wait_flags(c, FL_ACT, s + 1, kq * 9, 9);
            BAR();
            load(s + 1);
            if (s + 2 < NSEG && w == 0) nr = check_flags(c, FL_ACT, s + 2, kq * 9, 9);
        }
        store(s + 1);
        if (c.tid == 0) CTL[(s + 1) & 1] = nr;
        BAR();
        if (rdy) { if (pending >= 0 && c.tid == 0) set_flag(c, FL_P2, pending, word); pending = s; }
        rdy = CTL[(s + 1) & 1] != 0;
        STMPW(0, s, 4);
    }
}

__host__ __device__ inline void role_of(int b, int& role, int& id) {
    const int x = b & 7, k = b >> 3;
    if (x < 3) {
        const int n = k * 3 + x;
        if (n < 32) { role = 0; id = n; } else if (n < 64) { role = 1; id = n - 32; } else if (n < 82) { role = 2; id = n - 64; } else { role = 8; id = n - 82; }
    } else if (x == 3) { if (k < 18) { role = 5; id = k; } else { role = 3; id = k - 18; } }
    else if (x == 4) { if (k < 18) { role = 7; id = k; } else { role = 3; id = 14 + (k - 18); } }
    else if (x == 5) { if (k < 18) { role = 7; id = 18 + k; } else if (k < 22) { role = 3; id = 28 + (k - 18); } else { role = 4; id = k - 22; } }
    else if (x == 6) { if (k < 18) { role = 6; id = k; } else { role = 4; id = 10 + (k - 18); } }
    else { if (k < 18) { role = 6; id = 18 + k; } else if (k < 26) { role = 4; id = 24 + (k - 18); } else if (k < 28) { role = 8; id = 14 + (k - 26); } else { role = 9; id = 0; } }
}
__global__ void __launch_bounds__(512, 2) mega(Params p) {
    extern __shared__ __attribute__((aligned(16))) char smem[];
    LAS char* lds = (LAS char*)smem;
    Ctx c; c.ws = p.ws; c.r = __builtin_amdgcn_make_buffer_rsrc(p.ws, 0, WS_END, 0x00020000);
    c.tid = threadIdx.x; c.lane = threadIdx.x & 63; c.wave = __builtin_amdgcn_readfirstlane(threadIdx.x >> 6);
    c.lds = lds;
    const int b = blockIdx.x;
    int role, id;
    role_of(b, role, id);
    if (role == 0) role_sc(p, c, lds, id);
    else if (role == 1) role_vc(p, c, lds, id);
    else if (role == 2) role_rd(p, c, lds, id);
    else if (role == 3) role_qk(p, c, lds, id);
    else if (role == 4) role_vw(p, c, lds, id);
    else if (role == 5) { if (POST_LEVEL >= 1) role_pvw(p, c, lds, id); }
    else if (role == 6) { if (POST_LEVEL >= 3) role_ffn1(p, c, lds, id); }
    else if (role == 7) { if (POST_LEVEL >= 4) role_ffn2(p, c, lds, id); }
    else if (role == 8) role_ln(p, c, lds, id);
    STMP_DUMP();
}

constexpr int K0_MOD = 108, K0_INIT = 64;
__global__ void __launch_bounds__(256) k0(const bf16* cvec, const bf16* wmod, char* ws) {
    const int b = blockIdx.x, t = threadIdx.x;
    if (b < K0_MOD) {
        __shared__ float red[64][33];
        const int cg = t & 3, kg = t >> 2;
        float acc[8] = {0, 0, 0, 0, 0, 0, 0, 0};
#pragma unroll
        for (int i = 0; i < 9; ++i) {
            const int k = kg * 9 + i;
            const float cv = (float)cvec[k];
            const float sc = rbf(cv / (1.0f + __expf(-cv)));
            u4 wv = *(const u4*)(wmod + (size_t)k * 3456 + b * 32 + cg * 8);
            acc[0] += sc * lo16(wv.x); acc[1] += sc * hi16(wv.x); acc[2] += sc * lo16(wv.y); acc[3] += sc * hi16(wv.y);
            acc[4] += sc * lo16(wv.z); acc[5] += sc * hi16(wv.z); acc[6] += sc * lo16(wv.w); acc[7] += sc * hi16(wv.w);
        }
#pragma unroll
        for (int j = 0; j < 8; ++j) red[kg][cg * 8 + j] = acc[j];
        __syncthreads();
        if (t < 32) {
            float s = 0.f;
            for (int k = 0; k < 64; ++k) s += red[k][t];
            const int n = b * 32 + t;
            float v = rbf(s);
            if ((n >= 576 && n < 1152) || (n >= 2304 && n < 2880)) v = 1.0f + v;
            ((bf16*)(ws + OFF_MOD))[n] = (bf16)v;
        }
    } else {
        const int ib = b - K0_MOD;
        for (unsigned o = (ib * 256 + t) * 16; o < RING_END; o += K0_INIT * 256 * 16) {
            const unsigned v = o < ZERO_BYTES ? 0u : SENT;
            *(u4*)(ws + o) = (u4){v, v, v, v};
        }
    }
}

extern "C" void kernel_launch(void* const* d_in, const int* in_sizes, int n_in, void* d_out, int out_size, void* d_ws, size_t ws_size, hipStream_t stream) {
    (void)in_sizes; (void)n_in; (void)out_size; (void)ws_size;
    hipFuncSetAttribute((const void*)mega, hipFuncAttributeMaxDynamicSharedMemorySize, LDS_BYTES);
    k0<<<K0_MOD + K0_INIT, 256, 0, stream>>>((const bf16*)d_in[11], (const bf16*)d_in[12], (char*)d_ws);
    Params p;
    p.x = (const bf16*)d_in[0]; p.mem0 = (const bf16*)d_in[1]; p.Wq = (const bf16*)d_in[2]; p.Wk = (const bf16*)d_in[3]; p.Wv = (const bf16*)d_in[4]; p.Wo = (const bf16*)d_in[5];
    p.Wg = (const bf16*)d_in[8]; p.Wu = (const bf16*)d_in[9]; p.Wd = (const bf16*)d_in[10]; p.out = (bf16*)d_out; p.ws = (char*)d_ws;
    mega<<<256, 512, LDS_BYTES, stream>>>(p);
}
```

```cpp
#include <hip/hip_runtime.h>
#include <hip/hip_bf16.h>
#include <stdint.h>
#pragma clang fp contract(off)

#define DI __device__ __forceinline__
#define LAS __attribute__((address_space(3)))
typedef __bf16 bf16;
typedef __bf16 bf8 __attribute__((ext_vector_type(8)));
typedef __bf16 bf4 __attribute__((ext_vector_type(4)));
typedef __bf16 bf2 __attribute__((ext_vector_type(2)));
typedef __bf16 bf16v __attribute__((ext_vector_type(16)));
typedef float f4 __attribute__((ext_vector_type(4)));
typedef float f2 __attribute__((ext_vector_type(2)));
typedef unsigned u4 __attribute__((ext_vector_type(4)));
typedef unsigned u2 __attribute__((ext_vector_type(2)));
typedef short s4 __attribute__((ext_vector_type(4)));
typedef short s8 __attribute__((ext_vector_type(8)));
typedef __amdgpu_buffer_rsrc_t rsrc_t;

constexpr int D_ = 576, NH = 32, NSEG = 64, F_ = 4608;

constexpr unsigned OFF_TMO = 0;
constexpr unsigned OFF_FLAGS = 256;
enum { FL_H1 = 0, FL_K, FL_VW, FL_VWM, FL_P, FL_ATT, FL_H2, FL_ACT, FL_P2, NSEGFLAG, FL_MOD = NSEGFLAG, FL_FILL };
constexpr unsigned OFF_INIT = OFF_FLAGS + 1536;
constexpr unsigned OFF_EP = 2048, OFF_ACK = 8192;
constexpr unsigned OFF_MODT = 10240;
constexpr unsigned OFF_MOD = 16384;
constexpr unsigned OFF_NEAR = 256u << 10, NEAR_BYTES = 576u << 10;
constexpr unsigned M_SLOT = 32768, M_NSLOT = 4;
constexpr unsigned P_SLOT = 49152, P_NSLOT = 3, P_AREA = M_NSLOT * M_SLOT;
constexpr unsigned CAL_AREA = P_AREA + P_NSLOT * P_SLOT;
constexpr unsigned FLG_AREA = CAL_AREA + 16384, FLG_TYPE = 32768;
static_assert(FLG_AREA + NSEGFLAG * FLG_TYPE == NEAR_BYTES, "map");
#define FSIDE 0x1e1
constexpr unsigned W_SLOT = 36 * 16384, W_LEAD = 3;
constexpr unsigned OFF_W = OFF_NEAR + 2 * NEAR_BYTES + 16384;
static_assert(OFF_MOD + 8192 <= OFF_NEAR, "map");
constexpr unsigned OFF_H1 = OFF_W + 16384 + 64u * W_SLOT;
constexpr unsigned OFF_KF = OFF_H1 + 2048 * 1152;
constexpr unsigned OFF_VW = OFF_KF + 64 * 32 * 16384;
constexpr unsigned OFF_PR = OFF_VW + 64u * 36 * 32 * 1024;
constexpr unsigned OFF_PRM = OFF_PR + 64 * 32 * 2048;
constexpr unsigned OFF_ATT = OFF_PRM + 64 * 8 * 2048;
constexpr unsigned OFF_X1 = OFF_ATT + 64 * 36 * 1024;
constexpr unsigned OFF_H2 = OFF_X1 + 2048 * 1152;
constexpr unsigned OFF_ACT = OFF_H2 + 2048 * 1152;
constexpr unsigned OFF_P2 = OFF_ACT + 64 * 36 * 8192;
constexpr unsigned WS_END = OFF_P2 + 64 * 36 * 8192;
static_assert(WS_END <= (256u << 20), "workspace");
struct Near {
    unsigned wlo, nb, wb;
    __host__ __device__ void init(const void* ws) {
        wlo = (unsigned)(uintptr_t)ws;
        unsigned a = (wlo + OFF_NEAR + 16383u) & ~16383u;
        if ((a >> 21) != ((a + NEAR_BYTES - 1) >> 21)) a = ((a >> 21) + 1u) << 21;
        nb = a - wlo;
        wb = ((wlo + OFF_W + 16383u) & ~16383u) - wlo;
    }
    __host__ __device__ unsigned half(unsigned o, unsigned flip) const { return o + (((((wlo + o) >> 21) ^ flip) & 1u) << 13); }
};
__host__ __device__ inline unsigned spread(unsigned L) { return (L & 8191u) + ((L >> 13) << 14); }

#define FLAG_SLEEP 4
#define T_PNOM 268
#define T_OP 155
#define T_OW 152
#define FAST_FROM 0
#define F_PNOM 243
#define F_OP 142
#define F_OW 143
#define CAL_N 8
#define PR_SLEEP 0
#define DEFER_P 63
#define DEFER_W 63
#define VW_AT -1
#define PVW_DEFER 1
#define LN2_DEFER 1
#define COMPACT_K 1
#define COMPACT_VW 1
#define E0_KS 2
#define E1_KS 9
#define E2_KS 12
#define SPREAD 2
#define FFN_PRIO 1
#define FW 0
#define FFN2_IMM 61
#define PL_HOLD 300
#define TLB_WARM 2
#define EP_AT 130
#define PL_SLEEP 6
#define NT_CH true
#define NT_PRE true
#define T_CH 0
#define CLK_ADJ 1
#define CLK_T 127
#define CLK_F 373
#define LAT_K 0
#define LAT_0 400
#define S0 56
#define HELPR 7
#define CREW 1
#define T_IDLE 15300
#define EX_UP 0
#define EX_MAX 8
#define EX_DECAY 12
#define PRE_SIG 11
#define OWN_CLK 0
#define LOOK_AHEAD 3
#define TN_LEAD 6
#define E_N 14
#define E_X 0
#define G6 0
#define T_UP 5
#define T_HI 16
#define T_LO 6
#define T_WM 5
#define T_S 90
#define NT_W 1
#define T_W0 0
#define T_FFN 900
#define POST_LEVEL 9
constexpr unsigned SENT = 0xFFFFFFFFu;
constexpr unsigned SPIN_MAX = 1u << 20;
constexpr int LDS_BYTES = 160 * 1024;

struct Params {
    const bf16* x; const bf16* mem0; const bf16* Wq; const bf16* Wk; const bf16* Wv; const bf16* Wo;
    const bf16* Wg; const bf16* Wu; const bf16* Wd; const bf16* cvec; const bf16* wmod; bf16* out; char* ws;
};

DI float bf2f(bf16 v) { return (float)v; }
DI float rbf(float v) { return (float)(bf16)v; }
DI float lo16(unsigned w) { return __uint_as_float(w << 16); }
DI float hi16(unsigned w) { return __uint_as_float(w & 0xffff0000u); }
DI unsigned pk2(float a, float b) { bf2 r; r[0] = (bf16)a; r[1] = (bf16)b; return __builtin_bit_cast(unsigned, r); }
DI u4 ld16(rsrc_t r, unsigned voff, unsigned soff) { return __builtin_amdgcn_raw_buffer_load_b128(r, voff, soff, 16); }
DI u2 ld8(rsrc_t r, unsigned voff, unsigned soff) { return __builtin_amdgcn_raw_buffer_load_b64(r, voff, soff, 16); }
DI unsigned ld4(rsrc_t r, unsigned voff, unsigned soff) { return __builtin_amdgcn_raw_buffer_load_b32(r, voff, soff, 16); }
DI void st16(rsrc_t r, unsigned voff, unsigned soff, u4 v) { __builtin_amdgcn_raw_buffer_store_b128(v, r, voff + soff, 0, 16); }
DI void st8(rsrc_t r, unsigned voff, unsigned soff, u2 v) { __builtin_amdgcn_raw_buffer_store_b64(v, r, voff, soff, 16); }
DI void st4(rsrc_t r, unsigned voff, unsigned soff, unsigned v) { __builtin_amdgcn_raw_buffer_store_b32(v, r, voff, soff, 16); }
DI void st2(rsrc_t r, unsigned voff, unsigned soff, unsigned short v) { __builtin_amdgcn_raw_buffer_store_b16(v, r, voff, soff, 16); }
#define CFENCE() asm volatile("" ::: "memory")
#define DRAIN() asm volatile("s_waitcnt vmcnt(0)" ::: "memory")
#define BAR() asm volatile("s_waitcnt lgkmcnt(0)\n\ts_barrier" ::: "memory")
DI s4 trd(LAS char* p) { return __builtin_amdgcn_ds_read_tr16_b64_v4i16((LAS s4*)p); }
DI bf8 cat8(s4 a, s4 b) { return __builtin_bit_cast(bf8, __builtin_shufflevector(a, b, 0, 1, 2, 3, 4, 5, 6, 7)); }
DI f4 mfma(bf8 a, bf8 b, f4 c) { return __builtin_amdgcn_mfma_f32_16x16x32_bf16(a, b, c, 0, 0, 0); }
DI f4 smf(bf8 a, bf16v b, f4 c, int idx) { return __builtin_amdgcn_smfmac_f32_16x16x64_bf16(a, b, c, idx, 0, 0); }
DI unsigned mx4(u4 v) { return max(max(v.x, v.y), max(v.z, v.w)); }
DI bf8 asbf8(u4 v) { return __builtin_bit_cast(bf8, v); }

struct Ctx { char* ws; rsrc_t r; u4 srd; int tid, lane, wave; LAS char* lds; Near nr; unsigned flip, mb, pb, fa, fb, E, n0, n1; int blk; };
DI unsigned fspread(unsigned s) { return ((s >> 5) << 14) + ((s & 31u) << 8); }
DI unsigned fl_base(const Ctx& c, int type) { return (((FSIDE >> type) & 1u) ? c.fb : c.fa) + type * FLG_TYPE; }
DI unsigned fl_word(const Ctx& c, int type, int s, int w);
DI unsigned fl_off(const Ctx& c, int type, int s) { return type >= FL_MOD ? OFF_FLAGS + (type - FL_MOD) * 512 + s * 256 : fl_base(c, type) + fspread(s); }
DI void dma16(const Ctx& c, LAS char* dst, unsigned voff, unsigned soff = 0) {
    const unsigned m = __builtin_amdgcn_readfirstlane((unsigned)(uintptr_t)dst), so = __builtin_amdgcn_readfirstlane(soff);
    asm volatile("s_mov_b32 m0, %0\n\ts_nop 0\n\tbuffer_load_dwordx4 %1, %2, %3 offen sc1 lds" :: "s"(m), "v"(voff), "s"(c.srd), "s"(so));
}
DI unsigned fl_word(const Ctx& c, int type, int s, int w) {
    if (COMPACT_K && type == FL_K) return fl_base(c, type) + w * 256 + s * 4;
    if (COMPACT_VW && type == FL_VW) return fl_base(c, type) + (w >> 2) * 1024 + s * 16 + (w & 3) * 4;
    return fl_off(c, type, s) + w * 4;
}
DI unsigned lds_u(LAS unsigned* p) { return __builtin_amdgcn_readfirstlane(*(volatile LAS unsigned*)p); }
template <int N> DI void vm_wait() { asm volatile("s_waitcnt vmcnt(%0)" :: "n"(N) : "memory"); }
#define CNT(k, v) do {} while (0)
#define STMP(s, k) do {} while (0)
#define STMPV(s, k, v) do {} while (0)
#define STMPW(wv, s, k) do {} while (0)
#define STMP_DUMP() do {} while (0)
#define STMPMAX(s, k) do {} while (0)
#define STMPADD(s, k, v) do {} while (0)
#define STMPM(wv, s, k) do {} while (0)

DI bool spin_fail(const Ctx& c, unsigned& spins) {
    ++spins;
    if ((spins & 1023u) == 0u) {
        if (spins >= SPIN_MAX) { if (c.lane == 0) st4(c.r, OFF_TMO, 0, c.E); return true; }
        CFENCE();
        if (c.E != 0u && __builtin_amdgcn_readfirstlane(ld4(c.r, OFF_TMO, 0)) == c.E) return true;
    }
    return false;
}
DI void wait_flags(const Ctx& c, int type, int s, int w0, int n) {
    const unsigned base = fl_off(c, type, s) + w0 * 4;
    unsigned spins = 0;
    for (;;) {
        CFENCE();
        unsigned v = ld4(c.r, (c.lane < n ? c.lane : 0) * 4, base);
        if (__all(v == c.E)) break;
        __builtin_amdgcn_s_sleep(FLAG_SLEEP);
        if (spin_fail(c, spins)) break;
    }
    CFENCE();
}
DI bool check_flags(const Ctx& c, int type, int s, int w0, int n) {
    CFENCE();
    unsigned v = ld4(c.r, (c.lane < n ? c.lane : 0) * 4, fl_off(c, type, s) + w0 * 4);
    return __all(v == c.E);
}
DI void wait_mask(const Ctx& c, int type, int w, int s, unsigned long long& mask) {
    unsigned spins = 0;
    while (!((mask >> s) & 1ull)) {
        CFENCE();
        unsigned v = ld4(c.r, COMPACT_K ? c.lane * 4u : fspread(c.lane), fl_word(c, type, 0, w));
        mask = __ballot(v == c.E);
        if ((mask >> s) & 1ull) break;
        __builtin_amdgcn_s_sleep(FLAG_SLEEP);
        if (spin_fail(c, spins)) break;
    }
    CFENCE();
}
DI void wait_mask4(const Ctx& c, int type, int w, int s, unsigned long long& mask) {
    unsigned spins = 0;
    while (!((mask >> s) & 1ull)) {
        CFENCE();
        u4 v = ld16(c.r, COMPACT_VW ? c.lane * 16u : fspread(c.lane), fl_word(c, type, 0, w));
        mask = __ballot(v.x == c.E && v.y == c.E && v.z == c.E && v.w == c.E);
        if ((mask >> s) & 1ull) break;
        __builtin_amdgcn_s_sleep(FLAG_SLEEP);
        if (spin_fail(c, spins)) break;
    }
    CFENCE();
}
DI void set_flag(const Ctx& c, int type, int s, int w) { st4(c.r, 0, fl_word(c, type, s, w), c.E); }
DI void publish(const Ctx& c, int type, int s, int w) { DRAIN(); BAR(); if (c.tid == 0) set_flag(c, type, s, w); }

template <int NC, int NROWS> struct Panel {
    static constexpr int CPR = NC / 8, TOT = NROWS * CPR, IT = TOT / 512, RS = NC * 2 + 16;
    static_assert(TOT % 512 == 0, "panel");
    u4 v[IT];
    template <bool NT = true> DI void load(const bf16* W, int ldw, int tid) {
#pragma unroll
        for (int i = 0; i < IT; ++i) { const int n = tid + i * 512, r = n / CPR, ch = n % CPR; v[i] = (NT_W && NT) ? __builtin_nontemporal_load((const u4*)(W + (size_t)r * ldw + ch * 8)) : *(const u4*)(W + (size_t)r * ldw + ch * 8); }
    }
    DI void store(LAS char* lds, int tid) const {
#pragma unroll
        for (int i = 0; i < IT; ++i) { const int n = tid + i * 512, r = n / CPR, ch = n % CPR; *(LAS u4*)(lds + r * RS + ch * 16) = v[i]; }
    }
};
DI bf8 frag_dense(LAS char* lds, int RS, int row0, int cb, int lane) {
    const int g = lane >> 4, q = (lane & 15) >> 2, p = lane & 3;
    LAS char* a = lds + (row0 + 8 * g + q) * RS + cb + p * 8;
    return cat8(trd(a), trd(a + 4 * RS));
}
DI bf8 frag_perm(LAS char* lds, int RS, int row0, int cb, int lane) {
    const int g = lane >> 4, q = (lane & 15) >> 2, p = lane & 3;
    LAS char* a = lds + (row0 + 4 * g + q) * RS + cb + p * 8;
    return cat8(trd(a), trd(a + 16 * RS));
}
DI bf16v frag_sparse(LAS char* lds, int RS, int row0, int cb, int lane) {
    const int gb = lane >> 4, q = (lane & 15) >> 2, p = lane & 3;
    LAS char* a = lds + (row0 + 16 * (gb >> 1) + 8 * (q >> 1) + 4 * (gb & 1) + (q & 1)) * RS + cb + p * 8;
    s4 e0 = trd(a), e1 = trd(a + 2 * RS), e2 = trd(a + 32 * RS), e3 = trd(a + 34 * RS);
    s8 lo = __builtin_shufflevector(e0, e1, 0, 1, 2, 3, 4, 5, 6, 7), hi = __builtin_shufflevector(e2, e3, 0, 1, 2, 3, 4, 5, 6, 7);
    return __builtin_bit_cast(bf16v, __builtin_shufflevector(lo, hi, 0, 1, 2, 3, 4, 5, 6, 7, 8, 9, 10, 11, 12, 13, 14, 15));
}
DI unsigned tsw(unsigned a) { return a ^ ((a >> 2) & 64u); }
DI bf8 a_sparse(LAS char* T, int k0, int lane) {
    const int ga = lane >> 4, q = (lane & 15) >> 2, p = lane & 3;
    const unsigned a = (unsigned)(k0 + 16 * ga + 8 * (p >> 1) + q) * 16u;
    return cat8(trd(T + tsw(a) + (p & 1) * 8), trd(T + tsw(a + 64u) + (p & 1) * 8));
}
DI bf8 t_perm(LAS char* T, int k0, int lane) {
    const int g = lane >> 4, q = (lane & 15) >> 2, p = lane & 3;
    const unsigned a = (unsigned)(k0 + 4 * g + q) * 16u;
    return cat8(trd(T + tsw(a) + (p & 1) * 8), trd(T + tsw(a + 256u) + (p & 1) * 8));
}
DI float halves(float a, float b) {
    auto r = __builtin_amdgcn_permlane32_swap(__float_as_uint(a), __float_as_uint(b), false, false);
    return __uint_as_float(r[0]) + __uint_as_float(r[1]);
}
DI float xmax16(float v) { auto r = __builtin_amdgcn_permlane16_swap(__float_as_uint(v), __float_as_uint(v), false, false); return fmaxf(__uint_as_float(r[0]), __uint_as_float(r[1])); }
DI float xmax32(float v) { auto r = __builtin_amdgcn_permlane32_swap(__float_as_uint(v), __float_as_uint(v), false, false); return fmaxf(__uint_as_float(r[0]), __uint_as_float(r[1])); }
DI float xsum16(float v) { auto r = __builtin_amdgcn_permlane16_swap(__float_as_uint(v), __float_as_uint(v), false, false); return __uint_as_float(r[0]) + __uint_as_float(r[1]); }
DI float xsum32(float v) { auto r = __builtin_amdgcn_permlane32_swap(__float_as_uint(v), __float_as_uint(v), false, false); return __uint_as_float(r[0]) + __uint_as_float(r[1]); }
DI float xor8(float v) { return __uint_as_float(__builtin_amdgcn_update_dpp(0, __float_as_uint(v), 0x128  , 0xf, 0xf, false)); }
DI float vmax(float a, float b) { float r; asm("v_max_f32 %0, %1, %2" : "=v"(r) : "v"(a), "v"(b)); return r; }
DI float rmax16(float v) { auto r = __builtin_amdgcn_permlane16_swap(__float_as_uint(v), __float_as_uint(v), false, false); return vmax(__uint_as_float(r[0]), __uint_as_float(r[1])); }
DI float rmax32(float v) { auto r = __builtin_amdgcn_permlane32_swap(__float_as_uint(v), __float_as_uint(v), false, false); return vmax(__uint_as_float(r[0]), __uint_as_float(r[1])); }
DI unsigned cvtpk(float a, float b) { unsigned r; asm("v_cvt_pk_bf16_f32 %0, %1, %2" : "=v"(r) : "v"(a), "v"(b)); return r; }
DI float vmax3(float a, float b, float c) { float r; asm("v_max3_f32 %0, %1, %2, %3" : "=v"(r) : "v"(a), "v"(b), "v"(c)); return r; }
DI float max_xor8(float v) { float r; asm("s_nop 1\n\tv_max_f32_dpp %0, %1, %1 row_ror:8 row_mask:0xf bank_mask:0xf" : "=v"(r) : "v"(v)); return r; }
DI bf16v cat16(u4 a, u4 b) { return __builtin_bit_cast(bf16v, __builtin_shufflevector(a, b, 0, 1, 2, 3, 4, 5, 6, 7)); }
constexpr float SCALE = 0.08838834764831845f;
constexpr float LOG2E = 1.4426950408889634f;

DI unsigned now_t() { return (unsigned)__builtin_amdgcn_s_memrealtime(); }
DI void wait_until(unsigned t) {
    int d = (int)(t - now_t());
    if (d > 4000) return;
    while (d > 9) { __builtin_amdgcn_s_sleep(2); d = (int)(t - now_t()); }
    while (d > 0) d = (int)(t - now_t());
}
DI void scale2(float& a, float& b) { unsigned u = cvtpk(a, b); u = cvtpk(lo16(u) * SCALE, hi16(u) * SCALE); a = lo16(u); b = hi16(u); }
DI unsigned ep_chk(unsigned e, unsigned lo, unsigned hi) { return (e * 0x9E3779B1u) ^ (lo * 0x85EBCA6Bu) ^ (hi * 0xC2B2AE35u) ^ 0x1B873593u; }
DI u4 ep_ask(const Ctx& c) { CFENCE(); return ld16(c.r, 0, OFF_EP + (c.blk & 7) * 128); }
DI bool ep_take(Ctx& c, u4 v) {
    const unsigned e = __builtin_amdgcn_readfirstlane(v.x), lo = __builtin_amdgcn_readfirstlane(v.y), hi = __builtin_amdgcn_readfirstlane(v.z), ck = __builtin_amdgcn_readfirstlane(v.w);
    const unsigned dl = lo - c.n0;
    if (ck == ep_chk(e, lo, hi) && hi - c.n1 == (lo < c.n0 ? 1u : 0u) && dl <= now_t() - c.n0 + 1000u && e != 0u) { c.E = e; return true; }
    return false;
}
DI void ep_get(Ctx& c) {
    if (c.E == 0u) {
        unsigned spins = 0;
        while (!ep_take(c, ep_ask(c))) {
            __builtin_amdgcn_s_sleep(4);
            if (++spins >= SPIN_MAX) { c.E = 1u; break; }
        }
    }
    c.E = __builtin_amdgcn_readfirstlane(c.E);
    if (c.tid == 0) st4(c.r, 0, OFF_ACK + c.blk * 4, c.E);
}
DI void ep_master(Ctx& c) {
    unsigned E = c.n0 * 0x9E3779B1u + c.n1;
    if (E == 0u || E == SENT) E = 0x5A5A5A5Au;
    unsigned spins = 0;
    for (;;) {
        const unsigned long long ts = __builtin_amdgcn_s_memrealtime();
        const unsigned lo = (unsigned)ts, hi = (unsigned)(ts >> 32);
        st16(c.r, c.lane < 8 ? c.lane * 128u : 0x80000000u, OFF_EP, (u4){E, lo, hi, ep_chk(E, lo, hi)});
        CFENCE();
        if (spins < 6) STMPM(0, spins, 1);
        const u4 a = ld16(c.r, c.lane * 16, OFF_ACK);
        const int b = c.lane * 4;
        if (__all((a.x == E || b == c.blk) && (a.y == E || b + 1 == c.blk) && (a.z == E || b + 2 == c.blk) && (a.w == E || b + 3 == c.blk))) break;
        if (++spins >= SPIN_MAX) break;
    }
}
DI unsigned near_take(Ctx& c, unsigned v) {
    v = __builtin_amdgcn_readfirstlane(v); c.E = __builtin_amdgcn_readfirstlane(c.E);
    c.flip = (v >> 1) & 1u;
    const unsigned b = c.nr.half(c.nr.nb, c.flip);
    c.mb = b; c.pb = b + P_AREA; c.fa = b + FLG_AREA; c.fb = c.nr.half(c.nr.nb, c.flip ^ 1u) + FLG_AREA;
    return v;
}
DI unsigned near_setup(Ctx& c) {
    ep_get(c);
    unsigned v = 0, spins = 0;
    for (;;) {
        CFENCE();
        const u2 q = ld8(c.r, 0, OFF_INIT);
        if (__builtin_amdgcn_readfirstlane(q.x) == c.E) { v = __builtin_amdgcn_readfirstlane(q.y); break; }
        __builtin_amdgcn_s_sleep(16);
        if (spin_fail(c, spins)) break;
    }
    return near_take(c, v);
}
struct NearAsk { u4 ep; u2 ini; };
DI NearAsk near_ask(const Ctx& c) { NearAsk a; a.ep = ep_ask(c); a.ini = ld8(c.r, 0, OFF_INIT); return a; }
DI void near_setup(Ctx& c, const NearAsk& a) {
    if (ep_take(c, a.ep) && __builtin_amdgcn_readfirstlane(a.ini.x) == c.E) {
        if (c.tid == 0) st4(c.r, 0, OFF_ACK + c.blk * 4, c.E);
        near_take(c, a.ini.y);
    } else near_setup(c);
}
DI void near_measure(Ctx& c) {
    const unsigned cal = c.nr.nb + CAL_AREA, vo = c.lane < 8 ? c.lane * 16u : 0x80000000u;
    unsigned lo[2] = {~0u, ~0u};
#pragma unroll
    for (int q = 0; q < 4; ++q) st16(c.r, (c.lane >> 3) * 1024 + (q >> 1) * 8192 + (q & 1) * 512 + (c.lane & 7) * 16, cal, (u4){1u, 2u, 3u, 4u});
    DRAIN();
    { u4 v = ld16(c.r, vo, cal + 512), u = ld16(c.r, vo, cal + 8192 + 512); asm volatile("s_waitcnt vmcnt(0)" :: "v"(v), "v"(u) : "memory"); }
#pragma unroll 1
    for (int j = 0; j < CAL_N; ++j) {
        const unsigned side = (j ^ (j >> 1)) & 1;
        const unsigned long long t0 = __builtin_amdgcn_s_memtime();
        u4 v = ld16(c.r, vo, cal + (j >> 1) * 1024 + side * 8192);
        asm volatile("s_waitcnt vmcnt(0)" :: "v"(v) : "memory");
        const unsigned d = (unsigned)(__builtin_amdgcn_s_memtime() - t0);
        if (side) lo[1] = min(lo[1], d); else lo[0] = min(lo[0], d);
        STMPV(j >> 1, 1 + side, d);
    }
    const unsigned n13 = lo[1] < lo[0] ? 1u : 0u;
    const bool clear = lo[n13] * 5 < lo[n13 ^ 1u] * 4;
    const unsigned flip = n13 ^ (((c.nr.wlo + c.nr.nb) >> 21) & 1u);
    ep_get(c);
    if (c.lane == 0) st8(c.r, 0, OFF_INIT, (u2){c.E, (flip << 1) | (clear ? 0u : 4u) | (min(lo[n13], 4095u) << 8) | (min(lo[n13 ^ 1u], 4095u) << 20)});
}
constexpr int NRD = 18;
struct Sched { unsigned anchor, pnom, op, ow; };
struct MG { u4 a, b; unsigned va, vb, lo; };
template <int NB> DI void gather_init(const Ctx& c, MG& g, int gi) {
    g.lo = gi * (1024 + 16 * NB) + c.lane * 16;
    g.va = spread(g.lo);
    g.vb = c.lane < NB ? spread(g.lo + 1024u) : (c.lane < NB + 8 && c.wave == 0) ? spread(9216u + (c.lane & 7) * 16) : 0x80000000u;
}
DI void gather_issue(const Ctx& c, unsigned off, MG& g) {
    g.a = ld16(c.r, g.va, off);
    g.b = ld16(c.r, g.vb, off);
}
template <int NB> DI void gather_finish(const Ctx& c, unsigned off, MG& g, LAS char* T, Sched& sc, unsigned& fails) {
    if (__builtin_expect(__any(max(mx4(g.a), mx4(g.b)) == SENT), 0)) {
        unsigned spins = 0;
        for (;;) {
            ++fails; __builtin_amdgcn_s_sleep(2);
            CFENCE();
            g.a = ld16(c.r, g.va, off);
            g.b = ld16(c.r, g.vb, off);
            if (!__any(max(mx4(g.a), mx4(g.b)) == SENT) || spin_fail(c, spins)) break;
        }
    }
    *(LAS u4*)(T + tsw(g.lo)) = g.a;
    if (c.lane < NB) *(LAS u4*)(T + tsw(g.lo + 1024)) = g.b;
    sc.anchor = __builtin_amdgcn_readlane(g.b.x, NB); sc.pnom = __builtin_amdgcn_readlane(g.b.y, NB);
}
DI void watch_M(const Ctx& c, unsigned off, Sched& sc, unsigned& fails) {
    const unsigned vb = c.lane == 0 ? spread(9216u) : 0x80000000u;
    u4 b = ld16(c.r, vb, off);
    if (__builtin_expect(__any(mx4(b) == SENT), 0)) {
        unsigned spins = 0;
        for (;;) {
            ++fails; __builtin_amdgcn_s_sleep(2);
            CFENCE();
            b = ld16(c.r, vb, off);
            if (!__any(mx4(b) == SENT) || spin_fail(c, spins)) break;
        }
    }
    sc.anchor = __builtin_amdgcn_readlane(b.x, 0); sc.pnom = __builtin_amdgcn_readlane(b.y, 0); sc.op = __builtin_amdgcn_readlane(b.z, 0); sc.ow = __builtin_amdgcn_readlane(b.w, 0);
}
struct Mem0 {
    unsigned short v[9];
    DI void load(const bf16* mem0, int tid) {
#pragma unroll
        for (int i = 0; i < 9; ++i) v[i] = ((const unsigned short*)mem0)[tid + i * 512];
    }
    DI void store(LAS char* T, int tid) const {
#pragma unroll
        for (int i = 0; i < 9; ++i) { const int n = tid + i * 512, r = n / 576, k = n % 576; *(LAS unsigned short*)(T + tsw(k * 16) + r * 2) = v[i]; }
    }
};

DI void role_sc(const Params& p, Ctx& c, LAS char* lds, int h) {
    const int lane = c.lane, w = c.wave;
    bf16v wq[9], wk[9];
    Panel<128, 576> pa_, pb_;
    pa_.template load<NT_CH>(p.Wq + h * 128, 4096, c.tid); pb_.template load<NT_CH>(p.Wk + h * 128, 4096, c.tid);
    Mem0 m0_; m0_.load(p.mem0, c.tid);
    pa_.store(lds, c.tid); __syncthreads();
#pragma unroll
    for (int kb = 0; kb < 9; ++kb) wq[kb] = frag_sparse(lds, 272, kb * 64, w * 32, lane);
    __syncthreads();
    pb_.store(lds, c.tid); __syncthreads();
#pragma unroll
    for (int kb = 0; kb < 9; ++kb) wk[kb] = frag_sparse(lds, 272, kb * 64, w * 32, lane);
    __syncthreads();
    LAS char* T = lds;
    LAS char* TQ = lds + 9216;
    LAS char* TK = lds + 11264;
    LAS char* KS0 = lds + 13312;
    m0_.store(T, c.tid);
    near_setup(c);
    const unsigned pvo_ = ((lane >> 4) & 1) ? (((lane >> 4) == 1 && !(lane & 8)) ? spread(h * 640 + 512u + (lane & 7) * 16) : 0x80000000u) : spread(h * 640 + (unsigned)((lane >> 5) * 16 + (lane & 15)) * 16);
    if (w == 0) {
#pragma unroll
        for (int i = 0; i < (int)P_NSLOT; ++i) st16(c.r, pvo_, c.pb + i * P_SLOT, (u4){SENT, SENT, SENT, SENT});
        DRAIN();
        if (lane == 0) set_flag(c, FL_FILL, 1, h);
    }
    wait_flags(c, FL_FILL, 0, 0, NRD);
    unsigned long long kmask = 0;
    wait_mask(c, FL_K, h, 0, kmask);
    *(LAS u4*)(KS0 + w * 1024 + lane * 16) = ld16(c.r, lane * 16, OFF_KF + h * 16384 + w * 1024);
    *(LAS u4*)(KS0 + 8192 + w * 1024 + lane * 16) = ld16(c.r, lane * 16, OFF_KF + h * 16384 + 8192 + w * 1024);
    BAR();
    const int idx = (lane & 8) ? 0xEEEE : 0x4444;
    unsigned fails = 0;
    const bool gath = !G6 || (w != 1 && w != 2);
    Sched sc = {0, 0, 0, 0}; const int gi = G6 ? (w == 0 ? 0 : w - 2) : w;
    u4 aux[8];
    if (w <= 2) {
#pragma unroll
        for (int ks = 0; ks < 8; ++ks) aux[ks] = *(LAS u4*)(KS0 + ks * 1024 + lane * 16);
    }
    if (w == 0) __builtin_amdgcn_s_setprio(2);
    LAS unsigned* SCH = (LAS unsigned*)(lds + 13312 + 32768);
    for (int s = 0; s < NSEG; ++s) {
        STMPW(0, s, 0);
        LAS char* KS = KS0 + (s & 1) * 16384;
        const unsigned kfo = OFF_KF + ((s + 1) * 32 + h) * 16384 + (w & 3) * 4096 + lane * 16;
        LAS char* KN = KS0 + ((s + 1) & 1) * 16384;
        f4 aq = {0, 0, 0, 0}, ak = {0, 0, 0, 0};
        {
            bf8 af[9];
#pragma unroll
            for (int kb = 0; kb < 3; ++kb) af[kb] = a_sparse(T, kb * 64, lane);
#pragma unroll
            for (int kb = 0; kb < 9; ++kb) {
                __builtin_amdgcn_sched_barrier(0);
                if (kb + 3 < 9) af[kb + 3] = a_sparse(T, (kb + 3) * 64, lane);
                __builtin_amdgcn_sched_barrier(0);
                aq = smf(af[kb], wq[kb], aq, idx); ak = smf(af[kb], wk[kb], ak, idx);
            }
            __builtin_amdgcn_sched_barrier(0);
        }
        {
            float v0 = halves(aq[0], ak[0]), v1 = halves(aq[1], ak[1]), v2 = halves(aq[2], ak[2]), v3 = halves(aq[3], ak[3]);
            u2 o = {pk2(v0, v1), pk2(v2, v3)};
            const unsigned e = w * 16 + (lane & 15);
            *(LAS u2*)((lane < 32 ? TQ : TK) + tsw(e * 16) + ((lane >> 4) & 1) * 8) = o;
        }
        BAR();
        STMPW(0, s, 1);
        if (OWN_CLK && w != 0) { sc.anchor = lds_u(SCH); sc.pnom = lds_u(SCH + 1); }
        if (w == 0) {
            if (s < NSEG - 1) {
                bf8 qf[4], kf[4];
#pragma unroll
                for (int ks = 0; ks < 4; ++ks) qf[ks] = t_perm(TQ, ks * 32, lane);
#pragma unroll
                for (int ks = 0; ks < 4; ++ks) kf[ks] = t_perm(TK, ks * 32, lane);
                __builtin_amdgcn_sched_barrier(0);
                f4 s0 = {0, 0, 0, 0}, s1 = {0, 0, 0, 0}, s2 = {0, 0, 0, 0};
#pragma unroll
                for (int ks = 0; ks < 4; ++ks) { s0 = mfma(asbf8(aux[ks]), qf[ks], s0); s1 = mfma(asbf8(aux[4 + ks]), qf[ks], s1); }
#pragma unroll
                for (int ks = 0; ks < 4; ++ks) s2 = mfma(kf[ks], qf[ks], s2);
                const bool hf = lane & 8; const int g = lane >> 4;
                float v[6];
#pragma unroll
                for (int r = 0; r < 4; ++r) v[r] = hf ? s1[r] : s0[r];
                scale2(v[0], v[1]); scale2(v[2], v[3]);
                v[4] = hf ? s2[2] : s2[0]; v[5] = hf ? s2[3] : s2[1];
                scale2(v[4], v[5]);
                if (g >= 2) { v[4] = -INFINITY; v[5] = -INFINITY; }
                float m = vmax3(vmax3(v[0], v[1], v[2]), vmax3(v[3], v[4], v[5]), v[0]);
                m = max_xor8(m); m = rmax16(m); m = rmax32(m);
                const float mb = -m * LOG2E;
#pragma unroll
                for (int r = 0; r < 6; ++r) v[r] = __builtin_amdgcn_exp2f(__builtin_fmaf(v[r], LOG2E, mb));
                float sum = (v[0] + v[1]) + (v[2] + v[3]) + (v[4] + v[5]);
                sum += xor8(sum); sum = xsum16(sum); sum = xsum32(sum);
                const float inv = __builtin_amdgcn_rcpf(sum);
                const unsigned px = cvtpk(v[0] * inv, v[1] * inv), py = cvtpk(v[2] * inv, v[3] * inv), pm = cvtpk(v[4] * inv, v[5] * inv);
                auto rx = __builtin_amdgcn_permlane16_swap(px, px, false, false), ry = __builtin_amdgcn_permlane16_swap(py, py, false, false);
                auto rm = __builtin_amdgcn_permlane16_swap(pm, pm, false, false);
                const unsigned m23 = __float_as_uint(xor8(__uint_as_float(rm[0]))), m67 = __float_as_uint(xor8(__uint_as_float(rm[1])));
                u4 o = (g & 1) ? (u4){rm[0], m23, rm[1], m67} : (u4){rx[0], ry[0], rx[1], ry[1]};
                o.x = min(o.x, SENT - 1); o.y = min(o.y, SENT - 1); o.z = min(o.z, SENT - 1); o.w = min(o.w, SENT - 1);
                st16(c.r, pvo_, c.pb + (s % P_NSLOT) * P_SLOT, o);
                STMP(s, 2);
                if (s >= 1) st16(c.r, pvo_, c.pb + ((s + P_NSLOT - 1) % P_NSLOT) * P_SLOT, (u4){SENT, SENT, SENT, SENT});
            }
        } else if (w == 1 || w == 2) {
            const int qt = w - 1;
            if (PR_SLEEP) __builtin_amdgcn_s_sleep(PR_SLEEP);
            f4 s0 = {0, 0, 0, 0}, s1 = {0, 0, 0, 0}, sm = {0, 0, 0, 0};
#pragma unroll
            for (int ks = 0; ks < 4; ++ks) {
                bf8 km = t_perm(TK, ks * 32, lane);
                bf8 qf = asbf8(*(LAS u4*)(KS + 8192 + qt * 4096 + ks * 1024 + lane * 16));
                s0 = mfma(asbf8(aux[ks]), qf, s0); s1 = mfma(asbf8(aux[4 + ks]), qf, s1); sm = mfma(km, qf, sm);
            }
            const int g = lane >> 4, q = 16 * qt + (lane & 15);
            float v[12];
#pragma unroll
            for (int r = 0; r < 4; ++r) {
                v[r] = (4 * g + r <= q) ? rbf(rbf(s0[r]) * SCALE) : -INFINITY;
                v[4 + r] = (16 + 4 * g + r <= q) ? rbf(rbf(s1[r]) * SCALE) : -INFINITY;
                v[8 + r] = (g < 2) ? rbf(rbf(sm[r]) * SCALE) : -INFINITY;
            }
            float m = v[0];
#pragma unroll
            for (int r = 1; r < 12; ++r) m = fmaxf(m, v[r]);
            m = xmax16(m); m = xmax32(m);
            float sum = 0.f;
#pragma unroll
            for (int r = 0; r < 12; ++r) { v[r] = __builtin_amdgcn_exp2f((v[r] - m) * LOG2E); sum += v[r]; }
            sum = xsum16(sum); sum = xsum32(sum);
            const float inv = __builtin_amdgcn_rcpf(sum);
            u4 o = {pk2(v[0] * inv, v[1] * inv), pk2(v[4] * inv, v[5] * inv), pk2(v[2] * inv, v[3] * inv), pk2(v[6] * inv, v[7] * inv)};
            st16(c.r, lane * 16, OFF_PR + (((s) * 32 + h) * 2 + qt) * 1024, o);
            const unsigned mx = pk2(v[8] * inv, v[9] * inv), my = pk2(v[10] * inv, v[11] * inv);
            auto rx = __builtin_amdgcn_permlane16_swap(mx, mx, false, false), ry = __builtin_amdgcn_permlane16_swap(my, my, false, false);
            st16(c.r, lane < 16 ? (unsigned)((h & 3) * 16 + lane) * 16 : 0x80000000u, OFF_PRM + (((s) * 8 + (h >> 2)) * 2 + qt) * 1024, (u4){rx[0], ry[0], rx[1], ry[1]});
            if (s >= DEFER_P) { DRAIN(); if (lane == 0) set_flag(c, FL_P, s, h * 2 + qt); }
        }
        else if (w >= 4 && s + 1 < NSEG) {
            wait_mask(c, FL_K, h, s + 1, kmask);
#pragma unroll
            for (int i = 0; i < 4; ++i) dma16(c, KN + (w & 3) * 4096 + i * 1024, kfo + i * 1024);
            if (LOOK_AHEAD && !((kmask >> min(s + LOOK_AHEAD, NSEG - 1)) & 1ull)) { CFENCE(); const unsigned v = ld4(c.r, COMPACT_K ? c.lane * 4u : fspread(c.lane), fl_word(c, FL_K, 0, h)); kmask = __ballot(v == c.E); }
            DRAIN();
        }
        if (s == NSEG - 1) break;
        STMPW(0, s, 5);
        const unsigned mo = c.mb + ((s + 1) % M_NSLOT) * M_SLOT;
        if (!OWN_CLK && s >= 1 && w == 0) wait_until(sc.anchor + sc.pnom);
        BAR();
        if (OWN_CLK && s >= 1) wait_until(sc.anchor + sc.pnom);
        STMPW(0, s, 3);
        const unsigned f0 = fails;
        if (gath) {
            MG mg; gather_init<G6 ? 32 : 8>(c, mg, gi);
            gather_issue(c, mo, mg);
            if (w <= (G6 ? 0 : 2)) {
#pragma unroll
                for (int ks = 0; ks < 8; ++ks) aux[ks] = *(LAS u4*)(KN + ks * 1024 + lane * 16);
            }
            gather_finish<G6 ? 32 : 8>(c, mo, mg, T, sc, fails);
        } else {
#pragma unroll
            for (int ks = 0; ks < 8; ++ks) aux[ks] = *(LAS u4*)(KN + ks * 1024 + lane * 16);
        }
        if (OWN_CLK && c.tid == 0) { SCH[0] = sc.anchor; SCH[1] = sc.pnom; }
        if (fails != f0) STMPADD(s, 6, 1);
        STMPMAX(s, 4);
        BAR();
        if (s < DEFER_P && (w == 1 || w == 2)) { DRAIN(); if (lane == 0) set_flag(c, FL_P, s, h * 2 + w - 1); }
    }
    CNT(0, fails);
}

DI void role_vc(const Params& p, Ctx& c, LAS char* lds, int h) {
    const int lane = c.lane, w = c.wave;
    bf16v wv[9], wo[5][2];
    Panel<128, 576> pa_; Panel<576, 128> pb_;
    pa_.template load<NT_CH>(p.Wv + h * 128, 4096, c.tid); pb_.template load<NT_CH>(p.Wo + (size_t)h * 128 * 576, 576, c.tid);
    Mem0 m0_; m0_.load(p.mem0, c.tid);
    pa_.store(lds, c.tid); __syncthreads();
#pragma unroll
    for (int kb = 0; kb < 9; ++kb) wv[kb] = frag_sparse(lds, 272, kb * 64, w * 32, lane);
    __syncthreads();
    pb_.store(lds, c.tid); __syncthreads();
#pragma unroll
    for (int i = 0; i < 5; ++i) { const int ct = (w + 8 * i < 36) ? w + 8 * i : w;
        wo[i][0] = frag_sparse(lds, 1168, 0, ct * 32, lane); wo[i][1] = frag_sparse(lds, 1168, 64, ct * 32, lane); }
    __syncthreads();
    LAS char* T = lds;
    LAS char* TV = lds + 9216;
    LAS unsigned* SCH = (LAS unsigned*)(lds + 9216 + 2048);
    m0_.store(T, c.tid);
    near_setup(c);
    BAR();
    const int idx = (lane & 8) ? 0xEEEE : 0x4444;
    unsigned fails = 0;
    Sched sc = {0, 0, 0, 0}; MG mg; gather_init<8>(c, mg, w);
    const int col = lane & 15, R = lane >> 4;
    const int t1 = (R == 0) ? 0 : (R == 1) ? 2 : (R == 2) ? 1 : 3;
    const unsigned vo1 = (unsigned)((w + 8 * t1) * 16384 + h * 256 + col * 16), vo2 = (unsigned)((w + 32) * 16384 + h * 256 + col * 16);
    const bool on2 = w < 4 && R == 0;
    auto w_off = [&](int s, unsigned vo) { return c.nr.half(c.nr.wb + s * W_SLOT + vo, c.flip); };
    {
        const u4 z = {SENT, SENT, SENT, SENT};
#pragma unroll
        for (int i = 0; i < (int)W_LEAD; ++i) { st16(c.r, w_off(i, vo1), 0, z); st16(c.r, on2 ? w_off(i, vo2) : 0x80000000u, 0, z); }
        DRAIN(); BAR();
        if (c.tid == 0) set_flag(c, FL_FILL, 2, h);
        wait_flags(c, FL_FILL, 0, 0, NRD);
    }
    unsigned wa1 = w_off(0, vo1), wa2 = on2 ? w_off(0, vo2) : 0x80000000u;
    for (int s = 0; s < NSEG; ++s) {
        STMPW(0, s, 0);
        f4 av = {0, 0, 0, 0}, av2 = {0, 0, 0, 0};
        {
            bf8 af[9];
#pragma unroll
            for (int kb = 0; kb < 4; ++kb) af[kb] = a_sparse(T, kb * 64, lane);
#pragma unroll
            for (int kb = 0; kb < 9; ++kb) {
                __builtin_amdgcn_sched_barrier(0);
                if (kb + 4 < 9) af[kb + 4] = a_sparse(T, (kb + 4) * 64, lane);
                __builtin_amdgcn_sched_barrier(0);
                if (kb & 1) av2 = smf(af[kb], wv[kb], av2, idx); else av = smf(af[kb], wv[kb], av, idx);
            }
            __builtin_amdgcn_sched_barrier(0);
            av += av2;
        }
        {
            float v0 = halves(av[0], av[0]), v1 = halves(av[1], av[1]), v2 = halves(av[2], av[2]), v3 = halves(av[3], av[3]);
            u2 o = {pk2(v0, v1), pk2(v2, v3)};
            if (lane < 32) *(LAS u2*)(TV + tsw((w * 16 + (lane & 15)) * 16) + (lane >> 4) * 8) = o;
        }
        BAR();
        bf8 a0 = a_sparse(TV, 0, lane), a1 = a_sparse(TV, 64, lane);
        f4 d[5];
#pragma unroll
        for (int i = 0; i < 5; ++i) { d[i] = (f4){0, 0, 0, 0}; d[i] = smf(a0, wo[i][0], d[i], idx); d[i] = smf(a1, wo[i][1], d[i], idx); }
        u2 oa, ob, oc;
        { float v0 = halves(d[0][0], d[1][0]), v1 = halves(d[0][1], d[1][1]), v2 = halves(d[0][2], d[1][2]), v3 = halves(d[0][3], d[1][3]); oa = (u2){pk2(v0, v1), pk2(v2, v3)}; }
        { float v0 = halves(d[2][0], d[3][0]), v1 = halves(d[2][1], d[3][1]), v2 = halves(d[2][2], d[3][2]), v3 = halves(d[2][3], d[3][3]); ob = (u2){pk2(v0, v1), pk2(v2, v3)}; }
        auto sx = __builtin_amdgcn_permlane16_swap(oa.x, ob.x, false, false), sy = __builtin_amdgcn_permlane16_swap(oa.y, ob.y, false, false);
        u4 o1 = {min(sx[0], SENT - 1), min(sy[0], SENT - 1), min(sx[1], SENT - 1), min(sy[1], SENT - 1)};
        st16(c.r, wa1, 0, o1);
        { float v0 = halves(d[4][0], d[4][0]), v1 = halves(d[4][1], d[4][1]), v2 = halves(d[4][2], d[4][2]), v3 = halves(d[4][3], d[4][3]); oc = (u2){pk2(v0, v1), pk2(v2, v3)}; }
        auto tx = __builtin_amdgcn_permlane16_swap(oc.x, oc.x, false, false), ty = __builtin_amdgcn_permlane16_swap(oc.y, oc.y, false, false);
        u4 o2 = {min(tx[0], SENT - 1), min(ty[0], SENT - 1), min(tx[1], SENT - 1), min(ty[1], SENT - 1)};
        if (w < 4) st16(c.r, wa2, 0, o2);
        if (OWN_CLK && w != 0) { sc.anchor = lds_u(SCH); sc.pnom = lds_u(SCH + 1); }
        STMPW(0, s, 2);
        const bool defer = s < DEFER_W;
        if (!defer) {
            DRAIN(); BAR();
            if (c.tid == 0) set_flag(c, FL_VWM, s, h);
        }
        if (s == NSEG - 1) break;
        if (s + W_LEAD < NSEG - 1) {
            const u4 z = {SENT, SENT, SENT, SENT};
            st16(c.r, w_off(s + W_LEAD, vo1), 0, z); if (w < 4) st16(c.r, on2 ? w_off(s + W_LEAD, vo2) : 0x80000000u, 0, z);
        }
        const unsigned mo = c.mb + ((s + 1) % M_NSLOT) * M_SLOT;
        wa1 = w_off(s + 1, vo1); wa2 = on2 ? w_off(s + 1, vo2) : 0x80000000u;
        asm volatile("" : "+v"(wa1), "+v"(wa2));
        if (s >= 1) { if (OWN_CLK) wait_until(sc.anchor + sc.pnom); else { if (w == 0) wait_until(sc.anchor + sc.pnom); BAR(); } }
        STMPW(0, s, 5);
        const unsigned f0 = fails;
        gather_issue(c, mo, mg);
        gather_finish<8>(c, mo, mg, T, sc, fails);
        if (fails != f0) STMPADD(s, 7, 1);
        STMPMAX(s, 6);
        if (OWN_CLK && c.tid == 0) { SCH[0] = sc.anchor; SCH[1] = sc.pnom; }
        DRAIN(); BAR();
        if (defer && c.tid == 0) set_flag(c, FL_VWM, s, h);
    }
    CNT(0, fails);
}

DI void role_rd(const Params& p, Ctx& c, LAS char* lds, int id) {
    const int lane = c.lane, w = c.wave;
    LAS char* RED = lds;
    LAS unsigned* CTL = (LAS unsigned*)(lds + 8192);
    unsigned fails = 0, failsP = 0, failsW = 0, late = 0;
    unsigned long long vmask = 0;
    u4 vw[2][4];
    const unsigned nearw = near_setup(c);
    const int lat_adj = LAT_K * ((int)((nearw >> 8) & 4095u) - LAT_0) / 100;
    const bool slow = (nearw & 4u) != 0u;
    int trouble = 0; bool tired = false;
    u4 vq = {0, 0, 0, 0}; bool asked = false;
    const int la = lat_adj < 0 ? 0 : lat_adj > 4 ? 4 : lat_adj;
    int adj = 0, adj_o = 0; unsigned cy0 = 0, rt0_ = 0;
    const unsigned vo0 = (lane & 16) ? 0x80000000u : spread((unsigned)(2 * id * 16 + (lane & 15) + (lane >> 5) * 16) * 16);
    const unsigned vom = (id == NRD - 1 && lane >= 16 && lane < 24) ? spread(9216 + (lane - 16) * 16) : vo0;
    unsigned h_pn = slow ? T_PNOM : F_PNOM + 2 * la + E_X, h_op = slow ? T_OP : F_OP + la + E_X / 2, h_ow = slow ? T_OW : F_OW + la + E_X / 2;
    if (w == 0) {
        const unsigned vf = (id == NRD - 1 && lane >= 16 && lane < 24) ? spread(9216 + (lane - 16) * 16) : vo0;
#pragma unroll
        for (int i = 0; i < (int)M_NSLOT; ++i) st16(c.r, vf, c.mb + i * M_SLOT, (u4){SENT, SENT, SENT, SENT});
        DRAIN();
        if (lane == 0) set_flag(c, FL_FILL, 0, id);
    }
    Sched sc = {0, 0, 0, 0};
    wait_mask4(c, FL_VW, 4 * w, 0, vmask);
#pragma unroll
    for (int t = 0; t < 2; ++t)
#pragma unroll
        for (int i = 0; i < 4; ++i) vw[t][i] = ld16(c.r, lane * 16, OFF_VW + ((2 * id + t) * 32 + 4 * w + i) * 1024);
    const unsigned pl = 4 * w * 640 + (lane >> 5) * 640 + (((lane >> 4) & 1) * 16 + (lane & 15)) * 16;
    const unsigned pva = spread(pl), pvb = spread(pl + 1280);
    const unsigned mvo = (lane & 8) ? 0x80000000u : spread(4 * w * 640 + (lane >> 4) * 640 + 512 + (lane & 7) * 16);
    const int idx = (lane & 8) ? 0xEEEE : 0x4444;
    wait_flags(c, FL_FILL, 1, 0, 32); wait_flags(c, FL_FILL, 2, 0, 32);
    for (int s = 0; s < NSEG - 1; ++s) {
        STMPW(0, s, 0);
        if (w == 0) STMPV(s, 1, sc.anchor);
        u4 wm0, wm1, pa, pb, pmm;
        const unsigned fw0 = failsW, fp0 = failsP; (void)fw0; (void)fp0;
        unsigned ws0 = c.nr.half(c.nr.wb + s * W_SLOT + 2 * id * 16384, c.flip) + w * 1024, ws1 = c.nr.half(c.nr.wb + s * W_SLOT + (2 * id + 1) * 16384, c.flip) + w * 1024;
        unsigned psl = c.pb + (s % P_NSLOT) * P_SLOT;
        ws0 = __builtin_amdgcn_readfirstlane(ws0); ws1 = __builtin_amdgcn_readfirstlane(ws1); psl = __builtin_amdgcn_readfirstlane(psl);
        asm volatile("" : "+s"(ws0), "+s"(ws1), "+s"(psl));
        if (s >= 1) wait_until(sc.anchor + sc.ow);
        wm0 = ld16(c.r, lane * 16, ws0); wm1 = ld16(c.r, lane * 16, ws1);
        if (s >= 1) wait_until(sc.anchor + sc.op);
        STMPW(0, s, 3);
        pa = ld16(c.r, pva, psl); pb = ld16(c.r, pvb, psl); pmm = ld16(c.r, mvo, psl);
        if (__builtin_expect(__any(max(mx4(wm0), mx4(wm1)) == SENT), 0)) {
            unsigned spins = 0;
            for (;;) {
                ++failsW; __builtin_amdgcn_s_sleep(2);
                CFENCE();
                wm0 = ld16(c.r, lane * 16, ws0); wm1 = ld16(c.r, lane * 16, ws1);
                if (!__any(max(mx4(wm0), mx4(wm1)) == SENT) || spin_fail(c, spins)) break;
            }
        }
        if (__builtin_expect(__any(max(max(mx4(pa), mx4(pb)), mx4(pmm)) == SENT), 0)) {
            unsigned spins = 0;
            for (;;) {
                ++failsP; __builtin_amdgcn_s_sleep(2);
                CFENCE();
                pa = ld16(c.r, pva, psl); pb = ld16(c.r, pvb, psl); pmm = ld16(c.r, mvo, psl);
                if (!__any(max(max(mx4(pa), mx4(pb)), mx4(pmm)) == SENT) || spin_fail(c, spins)) break;
            }
        }
        STMPMAX(s, 4);
        if (failsW != fw0 || failsP != fp0) STMPADD(s, 2, (failsW != fw0 ? 1 : 0) + (failsP != fp0 ? 0x10000 : 0));
        {
            f4 d0 = {0, 0, 0, 0}, d1 = {0, 0, 0, 0};
            d0 = smf(asbf8(pa), cat16(vw[0][0], vw[0][1]), d0, idx); d1 = smf(asbf8(pa), cat16(vw[1][0], vw[1][1]), d1, idx);
            d0 = smf(asbf8(pb), cat16(vw[0][2], vw[0][3]), d0, idx); d1 = smf(asbf8(pb), cat16(vw[1][2], vw[1][3]), d1, idx);
            const f4 z4 = {0, 0, 0, 0};
            const f4 e0 = mfma(asbf8(pmm), asbf8(wm0), z4), e1 = mfma(asbf8(pmm), asbf8(wm1), z4);
            f4 r;
#pragma unroll
            for (int q = 0; q < 4; ++q) r[q] = halves(d0[q] + e0[q], d1[q] + e1[q]);
            *(LAS f4*)(RED + w * 1024 + lane * 16) = r;
        }
        BAR();
        if (w == 0) {
            const unsigned tn = id == NRD - 1 ? now_t() + TN_LEAD : 0u;
            f4 q[8];
#pragma unroll
            for (int j = 0; j < 8; ++j) q[j] = *(LAS f4*)(RED + j * 1024 + lane * 16);
            __builtin_amdgcn_sched_barrier(0);
            const f4 t = ((q[0] + q[1]) + (q[2] + q[3])) + ((q[4] + q[5]) + (q[6] + q[7]));
            const unsigned ox = pk2(t[0], t[1]), oy = pk2(t[2], t[3]);
            auto sx = __builtin_amdgcn_permlane16_swap(ox, ox, false, false), sy = __builtin_amdgcn_permlane16_swap(oy, oy, false, false);
            u4 o = {min(sx[0], SENT - 1), min(sy[0], SENT - 1), min(sx[1], SENT - 1), min(sy[1], SENT - 1)};
            const unsigned vo = vom;
            bool ontime = false;
            if (id == NRD - 1) {
                ontime = s >= 1 && (int)(sc.anchor + sc.pnom - tn) >= T_WM;
                const unsigned an = ontime ? sc.anchor + sc.pnom : tn + (s >= 1 ? T_S : 105);
                if (lane >= 16 && lane < 24) o = (u4){an == SENT ? 0u : an, h_pn, h_op, h_ow};
            }
            st16(c.r, vo, c.mb + ((s + 1) % M_NSLOT) * M_SLOT, o);
            STMPW(0, s, 7);
            if (s >= 1) st16(c.r, vo, c.mb + ((s + M_NSLOT - 1) % M_NSLOT) * M_SLOT, (u4){SENT, SENT, SENT, SENT});
            if (id == NRD - 1) {
                if (!ontime) { ++late; if (s >= 1) trouble = min(trouble + T_UP, T_HI + 8); } else if (trouble > 0) --trouble;
                if (trouble >= T_HI) tired = true; else if (trouble <= T_LO) tired = false;
            }
            if (CLK_ADJ && id == NRD - 1) {
                const unsigned cy = (unsigned)__builtin_amdgcn_s_memtime(), rt = now_t();
                if (s == 0) { cy0 = cy; rt0_ = rt; }
                else if (s >= 2) {
                    const int cpt = (int)(((cy - cy0) * 16u) / (rt - rt0_));
                    int a = (CLK_T * (CLK_F - cpt) + cpt / 2) / cpt;
                    a = a < -2 ? -2 : a > 12 ? 12 : a;
                    adj = a; adj_o = (a * 3) / 4;
                }
            }
            if (id == NRD - 1) {
                const bool relaxed = slow || tired;
                const int e_ = s + 2 < E_N ? E_X : 0;
                h_pn = relaxed ? T_PNOM : F_PNOM + adj + 2 * la + e_; h_op = relaxed ? T_OP : F_OP + adj_o + la + e_ / 2; h_ow = relaxed ? T_OW : F_OW + adj_o + la + e_ / 2;
            }
        }
        if (s + 1 < NSEG - 1) {
            auto vw_next = [&]() {
                if (asked) { vmask |= __ballot(vq.x == c.E && vq.y == c.E && vq.z == c.E && vq.w == c.E); asked = false; }
                wait_mask4(c, FL_VW, 4 * w, s + 1, vmask);
#pragma unroll
                for (int t = 0; t < 2; ++t)
#pragma unroll
                    for (int i = 0; i < 4; ++i) vw[t][i] = ld16(c.r, lane * 16, OFF_VW + (((s + 1) * 36 + 2 * id + t) * 32 + 4 * w + i) * 1024);
                if (LOOK_AHEAD && !((vmask >> min(s + 1 + LOOK_AHEAD, NSEG - 1)) & 1ull)) { CFENCE(); vq = ld16(c.r, COMPACT_VW ? c.lane * 16u : fspread(c.lane), fl_word(c, FL_VW, 0, 4 * w)); asked = true; }
            };
            if (w == 0) {
                if (s >= 1) wait_until(sc.anchor + sc.pnom);
                STMP(s, 5);
                watch_M(c, c.mb + ((s + 1) % M_NSLOT) * M_SLOT, sc, fails);
                STMP(s, 6);
                if (lane == 0) { CTL[0] = sc.anchor; CTL[1] = sc.pnom; CTL[2] = sc.op; CTL[3] = sc.ow; }
            }
            if (VW_AT >= 0 && s >= 1) { if (w != 0) wait_until(sc.anchor + sc.pnom + VW_AT - 64); vw_next(); }
            BAR();
            sc.anchor = __builtin_amdgcn_readfirstlane(CTL[0]); sc.pnom = __builtin_amdgcn_readfirstlane(CTL[1]); sc.op = __builtin_amdgcn_readfirstlane(CTL[2]); sc.ow = __builtin_amdgcn_readfirstlane(CTL[3]);
            if (!(VW_AT >= 0 && s >= 1)) vw_next();

        }
        BAR();
    }
    CNT(0, fails); CNT(1, failsP); CNT(2, late); CNT(3, failsW);
}

struct Seq {
    int first, from; bool help; unsigned wake;
    DI int nx(int s) const { return s >= from ? s + 2 : s + 1; }
    DI void rest(const Ctx& c) const { while ((unsigned)__builtin_amdgcn_s_memrealtime() - c.n0 < wake) __builtin_amdgcn_s_sleep(64); }
};
constexpr int IMG_BYTES = 36 * 1024;
struct RowsIn {
    static constexpr int NP_LO = 5, NP_HI = 4, BYTES = IMG_BYTES;
    unsigned off, vo[5];
    DI void init(const Ctx& c, unsigned o) {
        off = o;
#pragma unroll
        for (int i = 0; i < 5; ++i) { const int p = (c.wave + 8 * i) * 64 + c.lane, row = p / 72, ch = p % 72; vo[i] = (unsigned)(row * 72 + ((ch & ~7) | ((ch ^ (row >> 1)) & 7))) * 16; }
    }
    DI void fetch1(const Ctx& c, int s, LAS char* img, int i) const { if (i < 4 || c.wave < 4) dma16(c, img + (c.wave + 8 * i) * 1024, vo[i], off + s * 32 * 1152); }
    DI void fetch(const Ctx& c, int s, LAS char* img) const {
#pragma unroll
        for (int i = 0; i < 5; ++i) if (i < 4 || c.wave < 4) dma16(c, img + (c.wave + 8 * i) * 1024, vo[i], off + s * 32 * 1152);
    }
};
DI bf8 img_frag(LAS char* img, int t, int ks, int lane) {
    const int i = lane & 15, ch = (4 * (ks & 1) + (lane >> 4)) ^ (i >> 1);
    return asbf8(*(LAS u4*)(img + (16 * t + i) * 1152 + (ks >> 1) * 128 + ch * 16));
}
template <class Rd, class Mm> DI void ks_loop(Rd rd, Mm mm) {
    bf8 b[3][2];
    rd(0, b[0]); rd(1, b[1]);
#pragma unroll
    for (int ks = 0; ks < 18; ++ks) {
        __builtin_amdgcn_sched_barrier(0);
        if (ks + 2 < 18) rd(ks + 2, b[(ks + 2) % 3]);
        __builtin_amdgcn_sched_barrier(0);
        mm(ks, b[ks % 3]);
    }
    __builtin_amdgcn_sched_barrier(0);
}
template <int FIN, int NIN>
DI unsigned seg_peek(const Ctx& c, int s, int w0) {
    CFENCE();
    if (NIN == 1) { const u4 v = ld16(c.r, fspread(c.lane), fl_base(c, FIN)); return v.x == c.E && v.y == c.E && v.z == c.E && v.w == c.E; }
    return ld4(c.r, (c.lane < NIN ? c.lane : 0) * 4, fl_off(c, FIN, s) + w0 * 4) == c.E;
}
template <int NIN>
DI bool seg_eval(unsigned v, int s, unsigned long long& mask) {
    if (NIN == 1) { mask |= __ballot(v != 0u); return (mask >> s) & 1ull; }
    return __all(v != 0u);
}
template <int FIN, int NIN>
DI void seg_wait(const Ctx& c, int s, int w0, unsigned long long& mask) {
    unsigned spins = 0;
    while (!seg_eval<NIN>(seg_peek<FIN, NIN>(c, s, w0), s, mask)) {
        __builtin_amdgcn_s_sleep(FLAG_SLEEP);
        if (spin_fail(c, spins)) break;
    }
    CFENCE();
}
template <int FIN, int NIN, int FOUT, int NST, class Body>
DI void pipeline(const Ctx& c, LAS char* lds, unsigned in_off, int out_word, const Seq q, Body body) {
    LAS char* IMG = lds; LAS unsigned* CTL = (LAS unsigned*)(lds + 2 * IMG_BYTES);
    RowsIn in; in.init(c, in_off);
    unsigned long long mask = 0;
    auto look = [&](int s, bool on) { return ld4(c.r, (on && c.wave == 0) ? (unsigned)(fl_off(c, FIN, s) + (c.lane < NIN ? c.lane : 0) * 4) : 0xc0000000u, 0); };
    if (c.wave == 0) seg_wait<FIN, NIN>(c, q.first, 0, mask);
    BAR();
    STMPM(0, 6, 3);
    in.fetch(c, q.first, IMG);
    if (c.wave == 0) { const bool r = q.nx(q.first) < NSEG && seg_eval<NIN>(seg_peek<FIN, NIN>(c, q.nx(q.first), 0), q.nx(q.first), mask); if (c.lane == 0) CTL[0] = r; }
    DRAIN(); BAR();
    bool pre = lds_u(CTL) != 0; int pending = -1; unsigned f1 = 0;
    if (c.tid == 0) { CTL[4] = 0u; CTL[5] = 0u; CTL[6] = 0u; CTL[7] = 0u; CTL[8] = 0u; }
    for (int s_ = q.first, it_ = 0; s_ < NSEG; s_ = q.nx(s_), ++it_) {
        const int s = __builtin_amdgcn_readfirstlane(s_), it = __builtin_amdgcn_readfirstlane(it_), s1 = q.nx(s), s2 = q.nx(s1);
        pending = __builtin_amdgcn_readfirstlane(pending); pre = __builtin_amdgcn_readfirstlane((int)pre) != 0;
        STMPM(0, s, 0);
        const bool last = s1 >= NSEG;
        if (last) pre = false;
        bool late = false, flagged = false, full = false;
        unsigned f2 = 0;
        if (NIN > 1) f2 = look(s2, s2 < NSEG);
        if (pre && !SPREAD) in.fetch(c, s1, IMG + ((it + 1) & 1) * IMG_BYTES);
        auto tick = [&](int ks) {
            if (SPREAD && pre && ks >= 2 && (ks - 2) % SPREAD == 0 && (ks - 2) / SPREAD < 5) in.fetch1(c, s1, IMG + ((it + 1) & 1) * IMG_BYTES, (ks - 2) / SPREAD);
            if (NIN == 1 && PRE_SIG && ks == PRE_SIG && pending >= 0) {
                static_assert(PRE_SIG == 0 || (SPREAD == 2 && PRE_SIG >= 10), "all pieces are requested by then");
                if (!pre) vm_wait<0>(); else if (c.wave < 4) vm_wait<RowsIn::NP_LO>(); else vm_wait<RowsIn::NP_HI>();
                if (c.lane == 0 && __hip_atomic_fetch_add(CTL + 4, 1u, __ATOMIC_RELAXED, __HIP_MEMORY_SCOPE_WORKGROUP) == 7u) { CTL[4] = 0u; set_flag(c, FOUT, pending, out_word); }
                pending = -1;
            }
        };
        bool got = false;
        const unsigned fo = fl_off(c, FIN, s1) + (c.lane < NIN ? c.lane : 0) * 4;
        auto probe = [&]() { if (NIN > 1 && c.wave == 0 && !pre && !last && !got) { CFENCE(); f1 = ld4(c.r, fo, 0); } };
        auto early = [&](int step) {
            if (NIN == 1) return;
            const bool on = !pre && !last;
            if (step == 0) { if (c.wave == 0 && on) { CFENCE(); f1 = ld4(c.r, fo, 0); } return; }
            if ((step == 1) != (c.wave == 0)) return;
            if (step == 1 && on) { got = __all(f1 == c.E); CFENCE(); }
            else if (pending >= 0) {
                if (!pre) vm_wait<1>();
                else if (SPREAD) {
                    constexpr int n1 = (E1_KS - 2) / (SPREAD ? SPREAD : 1) + 1, n2 = (E2_KS - 2) / (SPREAD ? SPREAD : 1) + 1;
                    if (step == 1) vm_wait<1 + (n1 < 5 ? n1 : 5)>(); else if (n2 < 5 || c.wave >= 4) vm_wait<1 + (n2 < 4 ? n2 : 4)>(); else vm_wait<6>();
                }
                else if (c.wave < 4) vm_wait<RowsIn::NP_LO + 1>(); else vm_wait<RowsIn::NP_HI + 1>();
            }
            if (pending >= 0) {
                if (c.lane == 0 && __hip_atomic_fetch_add(CTL + 4, 1u, __ATOMIC_RELAXED, __HIP_MEMORY_SCOPE_WORKGROUP) == 7u) { CTL[4] = 0u; set_flag(c, FOUT, pending, out_word); }
                pending = -1;
            }
            if (on) {
                if (step == 1) { if (got && c.lane == 0) CTL[5] = s1; }
                else got = lds_u(CTL + 5) == (unsigned)s1;
                if (got) in.fetch(c, s1, IMG + ((it + 1) & 1) * IMG_BYTES);
            }
        };
        auto mid = [&]() {
            if (NIN == 1) {
                if (pending >= 0) { if (!pre) vm_wait<0>(); else if (c.wave < 4) vm_wait<RowsIn::NP_LO>(); else vm_wait<RowsIn::NP_HI>(); }
                BAR();
                if (pending >= 0 && c.tid == 0) set_flag(c, FOUT, pending, out_word);
                flagged = pending >= 0 && c.wave == 0;
                pending = -1;
            } else {
                if (pre || got) DRAIN();
                if (got && c.lane == 0) __hip_atomic_fetch_add(CTL + 6 + it % 3, 1u, __ATOMIC_RELAXED, __HIP_MEMORY_SCOPE_WORKGROUP);
                if (c.wave == 0) {
                    bool q = got;
                    if (!pre && !got && !last) { CFENCE(); q = __all(f1 == c.E); }
                    const bool nr = s2 < NSEG && __all(f2 == c.E);
                    if (c.lane == 0) { CTL[9 + (it & 1)] = q; CTL[(it + 1) & 1] = nr; CTL[6 + (it + 1) % 3] = 0u; }
                }
                BAR();
                const unsigned cn = lds_u(CTL + 6 + it % 3), cq = lds_u(CTL + 9 + (it & 1));
                full = pre || cn == 8u;
                late = !full && cq != 0;
                if (late && !got) in.fetch(c, s1, IMG + ((it + 1) & 1) * IMG_BYTES);
            }
        };
        STMPW(0, s, 1);
        body(s, it, IMG + (it & 1) * IMG_BYTES, probe, mid, early, tick);
        STMPW(0, s, 2);
        if (full) { pending = s; STMPW(0, s, 3); }
        else if (!pre && !late) {
            if (c.wave == 0) { const bool n1 = !last && seg_eval<NIN>(seg_peek<FIN, NIN>(c, s1, 0), s1, mask); if (c.lane == 0) CTL[3] = n1; }
            DRAIN(); BAR();
            if (c.tid == 0) set_flag(c, FOUT, s, out_word);
            STMPW(0, s, 3);
            if (last) break;
            if (lds_u(CTL + 3) == 0) { if (c.wave == 0) seg_wait<FIN, NIN>(c, s1, 0, mask); BAR(); }
            in.fetch(c, s1, IMG + ((it + 1) & 1) * IMG_BYTES);
            if (c.wave == 0) { const bool nr = s2 < NSEG && seg_eval<NIN>(seg_peek<FIN, NIN>(c, s2, 0), s2, mask); if (c.lane == 0) CTL[(it + 1) & 1] = nr; }
            DRAIN(); BAR();
        } else {
            const bool imm = NIN == 1 ? s < 4 : false;
            if (NIN == 1 && c.wave == 0) {
                bool nr = false;
                if (s2 < NSEG) nr = ((mask >> s2) & 1ull) || seg_eval<NIN>(seg_peek<FIN, NIN>(c, s2, 0), s2, mask);
                if (c.lane == 0) CTL[(it + 1) & 1] = nr;
            }
            if (imm) DRAIN(); else if (flagged) vm_wait<NST + 1>(); else vm_wait<NST>();
            BAR();
            if (imm && c.tid == 0) set_flag(c, FOUT, s, out_word);
            pending = imm ? -1 : s;
            STMPW(0, s, 3);
        }
        pre = lds_u(CTL + ((it + 1) & 1)) != 0;
        STMPW(0, s, 4);
    }
}

DI void role_qk(const Params& p, Ctx& c, LAS char* lds, int h) {
    const int lane = c.lane, w = c.wave;
    bf8 wf[2][18];
    Panel<128, 576> pa_, pb_;
    pa_.template load<NT_PRE>(p.Wk + h * 128, 4096, c.tid); pb_.template load<NT_PRE>(p.Wq + h * 128, 4096, c.tid);
    pa_.store(lds, c.tid); __syncthreads();
    const NearAsk na = near_ask(c);
    __builtin_amdgcn_sched_barrier(0);
    STMPM(0, 1, 3);
    if (w < 4) {
#pragma unroll
        for (int t = 0; t < 2; ++t)
#pragma unroll
            for (int ks = 0; ks < 18; ++ks) wf[t][ks] = frag_dense(lds, 272, ks * 32, (2 * w + t) * 32, lane);
    }
    __syncthreads();
    STMPM(0, 2, 3);
    pb_.store(lds, c.tid); __syncthreads();
    STMPM(0, 3, 3);
    if (w >= 4) {
#pragma unroll
        for (int t = 0; t < 2; ++t)
#pragma unroll
            for (int ks = 0; ks < 18; ++ks) wf[t][ks] = frag_dense(lds, 272, ks * 32, (2 * (w - 4) + t) * 32, lane);
    }
    __syncthreads();
    STMPM(0, 4, 3);
    near_setup(c, na);
    STMPM(0, 5, 3);
    pipeline<FL_H1, 1, FL_K, 2>(c, lds, OFF_H1, h, Seq{0, NSEG, false, 0u}, [&](int s, int it, LAS char* IMG, auto probe, auto mid, auto early, auto tick) {
        f4 acc[2][2];
#pragma unroll
        for (int a = 0; a < 2; ++a) for (int t = 0; t < 2; ++t) acc[a][t] = (f4){0, 0, 0, 0};
        ks_loop([&](int ks, bf8 (&b)[2]) { tick(ks); b[0] = img_frag(IMG, 0, ks, lane); b[1] = img_frag(IMG, 1, ks, lane); },
                [&](int ks, bf8 (&b)[2]) {
#pragma unroll
                    for (int t = 0; t < 2; ++t) { acc[0][t] = mfma(wf[0][ks], b[t], acc[0][t]); acc[1][t] = mfma(wf[1][ks], b[t], acc[1][t]); } });
        mid();
#pragma unroll
        for (int t = 0; t < 2; ++t) {
            u4 o = {pk2(acc[0][t][0], acc[0][t][1]), pk2(acc[0][t][2], acc[0][t][3]), pk2(acc[1][t][0], acc[1][t][1]), pk2(acc[1][t][2], acc[1][t][3])};
            st16(c.r, (w >> 2) * 8192 + t * 4096 + (w & 3) * 1024 + lane * 16, OFF_KF + (s * 32 + h) * 16384, o);
        }
    });
}

DI void role_vw(const Params& p, Ctx& c, LAS char* lds, int h) {
    const int lane = c.lane, w = c.wave;
    bf8 wv[18], wo[5][4];
    Panel<128, 576> pa_; Panel<576, 128> pb_;
    pa_.template load<NT_PRE>(p.Wv + h * 128, 4096, c.tid); pb_.template load<NT_PRE>(p.Wo + (size_t)h * 128 * 576, 576, c.tid);
    pa_.store(lds, c.tid); __syncthreads();
    const NearAsk na = near_ask(c);
    __builtin_amdgcn_sched_barrier(0);
#pragma unroll
    for (int ks = 0; ks < 18; ++ks) wv[ks] = frag_dense(lds, 272, ks * 32, w * 32, lane);
    __syncthreads();
    pb_.store(lds, c.tid); __syncthreads();
#pragma unroll
    for (int i = 0; i < 5; ++i) { const int ct = (w + 8 * i < 36) ? w + 8 * i : w;
#pragma unroll
        for (int ks = 0; ks < 4; ++ks) wo[i][ks] = frag_perm(lds, 1168, ks * 32, ct * 32, lane); }
    __syncthreads();
    LAS char* VI = lds + 2 * IMG_BYTES + 64;
    near_setup(c, na);
    pipeline<FL_H1, 1, FL_VW, 4>(c, lds, OFF_H1, h, Seq{0, NSEG, false, 0u}, [&](int s, int it, LAS char* IMG, auto probe, auto mid, auto early, auto tick) {
        f4 acc[2] = {{0, 0, 0, 0}, {0, 0, 0, 0}};
        ks_loop([&](int ks, bf8 (&b)[2]) { tick(ks); b[0] = img_frag(IMG, 0, ks, lane); b[1] = img_frag(IMG, 1, ks, lane); },
                [&](int ks, bf8 (&b)[2]) { acc[0] = mfma(wv[ks], b[0], acc[0]); acc[1] = mfma(wv[ks], b[1], acc[1]); });
#pragma unroll
        for (int t = 0; t < 2; ++t) { u2 o = {pk2(acc[t][0], acc[t][1]), pk2(acc[t][2], acc[t][3])}; *(LAS u2*)(VI + t * 4096 + (w >> 1) * 1024 + lane * 16 + (w & 1) * 8) = o; }
        mid();
        bf8 a[2][4];
#pragma unroll
        for (int t = 0; t < 2; ++t)
#pragma unroll
            for (int ks = 0; ks < 4; ++ks) a[t][ks] = asbf8(*(LAS u4*)(VI + t * 4096 + ks * 1024 + lane * 16));
#pragma unroll
        for (int i = 0; i < 5; ++i) {
            const int ct = w + 8 * i;
            if (i < 4 || w < 4) {
                f4 d0 = {0, 0, 0, 0}, d1 = {0, 0, 0, 0};
#pragma unroll
                for (int ks = 0; ks < 4; ++ks) { d0 = mfma(a[0][ks], wo[i][ks], d0); d1 = mfma(a[1][ks], wo[i][ks], d1); }
                u4 o = {pk2(d0[0], d0[1]), pk2(d1[0], d1[1]), pk2(d0[2], d0[3]), pk2(d1[2], d1[3])};
                st16(c.r, lane * 16, OFF_VW + (((s) * 36 + ct) * 32 + h) * 1024, o);
            }
        }
    });
}

DI void role_pvw(const Params& p, Ctx& c, LAS char* lds, int id, const Seq q) {
    const int lane = c.lane, w = c.wave;
    LAS char* RED = lds;
    LAS unsigned* CTL = (LAS unsigned*)(lds + 65536);
    u4 vwa[2][4], vwb[2][4]; unsigned long long vmask = 0;
    auto vw_load = [&](int s, u4 (&vw)[2][4]) {
#pragma unroll
        for (int tl = 0; tl < 2; ++tl)
#pragma unroll
            for (int i = 0; i < 4; ++i) vw[tl][i] = ld16(c.r, lane * 16, OFF_VW + ((s * 36 + 2 * id + tl) * 32 + 4 * w + i) * 1024);
    };
    auto look = [&](int s) {
        CFENCE();
        const unsigned a = ld4(c.r, lane * 4, fl_off(c, FL_P, s)), b = ld4(c.r, (lane & 31) * 4, fl_off(c, FL_VWM, s));
        return (bool)__all(a == c.E && b == c.E);
    };
    if (c.tid == 0) CTL[0] = 0u;
    int pending = -1;
    near_setup(c);
    BAR();
    wait_mask4(c, FL_VW, 4 * w, q.first, vmask);
    vw_load(q.first, vwa);
    auto step = [&](int s, int par, u4 (&vw)[2][4], u4 (&vn)[2][4]) {
        const int s1 = q.nx(s);
        STMPW(0, s, 0);
        if (w == 0 && lds_u(CTL) == 0u) {
            unsigned spins = 0;
            while (!look(s)) { __builtin_amdgcn_s_sleep(FLAG_SLEEP); if (spin_fail(c, spins)) break; }
            CFENCE();
        }
        if (s1 < NSEG) wait_mask4(c, FL_VW, 4 * w, s1, vmask);
        BAR();
        STMPM(0, s, 2);
        u4 pr[4][2], pm[2], wm[2];
#pragma unroll
        for (int i = 0; i < 4; ++i) for (int t = 0; t < 2; ++t) pr[i][t] = ld16(c.r, lane * 16, OFF_PR + ((s * 32 + 4 * w + i) * 2 + t) * 1024);
#pragma unroll
        for (int t = 0; t < 2; ++t) { pm[t] = ld16(c.r, lane * 16, OFF_PRM + ((s * 8 + w) * 2 + t) * 1024); wm[t] = ld16(c.r, lane * 16, c.nr.half(c.nr.wb + s * W_SLOT + (2 * id + t) * 16384, c.flip) + w * 1024); }
        const bool on = PVW_DEFER && w == 0 && s1 < NSEG;
        const unsigned fa = ld4(c.r, on ? fl_off(c, FL_P, s1) + lane * 4 : 0xc0000000u, 0), fb = ld4(c.r, on ? fl_off(c, FL_VWM, s1) + (lane & 31) * 4 : 0xc0000000u, 0);
        __builtin_amdgcn_sched_barrier(0);
        if (w < 2 && pending >= 0) { vm_wait<22>(); if (lane == 0) set_flag(c, FL_ATT, pending, 2 * id + w); }
        pending = -1;
        __builtin_amdgcn_sched_barrier(0);
        LAS char* R = RED + par * 32768;
#pragma unroll
        for (int tl = 0; tl < 2; ++tl)
#pragma unroll
            for (int t = 0; t < 2; ++t) {
                f4 d = {0, 0, 0, 0};
#pragma unroll
                for (int i = 0; i < 4; ++i) d = mfma(asbf8(vw[tl][i]), asbf8(pr[i][t]), d);
                d = mfma(asbf8(wm[tl]), asbf8(pm[t]), d);
                *(LAS f4*)(R + w * 4096 + tl * 2048 + t * 1024 + lane * 16) = d;
            }
        if (w == 0) { const bool q = on && __all(fa == c.E && fb == c.E); if (lane == 0) CTL[1] = q; }
        BAR();
        const bool behind = lds_u(CTL + 1) != 0u;
        if (w < 2) {
            f4 t0 = *(LAS f4*)(R + w * 1024 + lane * 16), t1 = *(LAS f4*)(R + 2048 + w * 1024 + lane * 16);
#pragma unroll
            for (int j = 1; j < 8; ++j) { t0 += *(LAS f4*)(R + j * 4096 + w * 1024 + lane * 16); t1 += *(LAS f4*)(R + j * 4096 + 2048 + w * 1024 + lane * 16); }
            auto sx = __builtin_amdgcn_permlane16_swap(pk2(t0[0], t0[1]), pk2(t1[0], t1[1]), false, false);
            auto sy = __builtin_amdgcn_permlane16_swap(pk2(t0[2], t0[3]), pk2(t1[2], t1[3]), false, false);
            const int Rw = lane >> 4;
            st16(c.r, (16 * w + (lane & 15)) * 64 + ((Rw & 1) * 2 + (Rw >> 1)) * 16, OFF_ATT + (s * 18 + id) * 2048, (u4){sx[0], sy[0], sx[1], sy[1]});
            STMPW(0, s, 3);
            if (behind) { pending = s; if (c.tid == 0) CTL[0] = 1u; }
            else {
                bool n1 = false;
                if (w == 0 && s1 < NSEG) n1 = look(s1);
                DRAIN();
                if (lane == 0) { set_flag(c, FL_ATT, s, 2 * id + w); if (w == 0) CTL[0] = n1; }
            }
            STMPW(0, s, 4);
        }
        vw_load(s1 < NSEG ? s1 : s, vn);
    };
    for (int s = q.first; s < NSEG;) {
        step(s, 0, vwa, vwb); s = q.nx(s);
        if (s >= NSEG) break;
        step(s, 1, vwb, vwa); s = q.nx(s);
    }
}

DI float wave_sum(float v) {
    v += __uint_as_float(__builtin_amdgcn_update_dpp(0, __float_as_uint(v), 0x121, 0xf, 0xf, false));
    v += __uint_as_float(__builtin_amdgcn_update_dpp(0, __float_as_uint(v), 0x122, 0xf, 0xf, false));
    v += __uint_as_float(__builtin_amdgcn_update_dpp(0, __float_as_uint(v), 0x124, 0xf, 0xf, false));
    v += xor8(v);
    v = xsum16(v); v = xsum32(v);
    return v;
}
DI void unpack8(u4 v, float (&x)[9]) { x[0] = lo16(v.x); x[1] = hi16(v.x); x[2] = lo16(v.y); x[3] = hi16(v.y); x[4] = lo16(v.z); x[5] = hi16(v.z); x[6] = lo16(v.w); x[7] = hi16(v.w); }
DI void row_load(const bf16* rowp, int lane, float (&x)[9]) {
    unpack8(*(const u4*)(rowp + 8 * lane), x);
    x[8] = (float)rowp[512 + lane];
}
DI void row_store(const Ctx& c, unsigned off, const float (&y)[9], LAS char* scr) {
    st16(c.r, c.lane * 16, off, (u4){pk2(y[0], y[1]), pk2(y[2], y[3]), pk2(y[4], y[5]), pk2(y[6], y[7])});
    *(LAS bf16*)(scr + c.lane * 2) = (bf16)y[8];
    CFENCE();
    const u4 t = *(LAS u4*)(scr + (c.lane & 7) * 16);
    CFENCE();
    st16(c.r, c.lane < 8 ? 1024u + c.lane * 16 : 0x80000000u, off, t);
}
DI void rbf9(float (&x)[9]) {
#pragma unroll
    for (int j = 0; j < 8; j += 2) { const unsigned u = pk2(x[j], x[j + 1]); x[j] = lo16(u); x[j + 1] = hi16(u); }
    x[8] = rbf(x[8]);
}
DI void ln_mod(float (&x)[9], const float (&cc)[9], const float (&ss)[9]) {
    float t = 0.f;
#pragma unroll
    for (int j = 0; j < 9; ++j) t += x[j];
    const float mean = wave_sum(t) * (1.0f / 576.0f);
    t = 0.f;
#pragma unroll
    for (int j = 0; j < 9; ++j) { const float d = x[j] - mean; t += d * d; }
    const float var = wave_sum(t) * (1.0f / 576.0f);
    const float mb = rbf(mean), den = rbf(sqrtf(rbf(rbf(var) + 1e-5f))), rden = 1.0f / den;
#pragma unroll
    for (int j = 0; j < 9; ++j) x[j] = x[j] - mb;
    rbf9(x);
#pragma unroll
    for (int j = 0; j < 9; ++j) {
        const float q = x[j] * rden;
        x[j] = __builtin_fmaf(__builtin_fmaf(-q, den, x[j]), rden, q);
    }
    rbf9(x);
#pragma unroll
    for (int j = 0; j < 9; ++j) x[j] = x[j] * cc[j];
    rbf9(x);
#pragma unroll
    for (int j = 0; j < 9; ++j) x[j] = x[j] + ss[j];
}
DI void hold_back(unsigned ticks) { const unsigned t0 = now_t(); while (now_t() - t0 < ticks) __builtin_amdgcn_s_sleep(16); }
constexpr int NPREP = 24;
DI void mod_row(const Ctx& c, int r, float (&x)[9]) {
    unpack8(ld16(c.r, c.lane * 16, OFF_MOD + r * 1152), x);
    x[8] = __uint_as_float((unsigned)__builtin_amdgcn_raw_buffer_load_b16(c.r, (512 + c.lane) * 2, OFF_MOD + r * 1152, 16) << 16);
}
DI void mod_block(const Params& p, const Ctx& c, LAS char* lds, int cb, const u4 (&wv)[9], const float (&sc)[9]) {
    const int t = c.tid, cg = t & 7, kg = t >> 3;
    LAS float* red = (LAS float*)(lds + 4096);
    LAS bf16* stg = (LAS bf16*)(lds + 2048);
    float acc[8] = {0, 0, 0, 0, 0, 0, 0, 0};
#pragma unroll
    for (int i = 0; i < 9; ++i) {
        const u4 v = wv[i];
        acc[0] += sc[i] * lo16(v.x); acc[1] += sc[i] * hi16(v.x); acc[2] += sc[i] * lo16(v.y); acc[3] += sc[i] * hi16(v.y);
        acc[4] += sc[i] * lo16(v.z); acc[5] += sc[i] * hi16(v.z); acc[6] += sc[i] * lo16(v.w); acc[7] += sc[i] * hi16(v.w);
    }
#pragma unroll
    for (int q = 0; q < 8; ++q) red[kg * 65 + cg * 8 + q] = acc[q];
    BAR();
    if (t < 64) {
        float sum = 0.f;
        for (int k = 0; k < 64; ++k) sum += red[k * 65 + t];
        const int n = cb * 64 + t;
        float v = rbf(sum);
        if ((n >= 576 && n < 1152) || (n >= 2304 && n < 2880)) v = 1.0f + v;
        stg[t] = (bf16)v;
        CFENCE();
        u4 o = *(LAS u4*)(lds + 2048 + (t & 7) * 16);
        if (cb < 18) {
            const u2 d = *(LAS u2*)(lds + 2048 + (t & 15) * 8);
            st16(c.r, t < 16 ? (unsigned)(cb * 256 + t * 16) : 0x80000000u, OFF_MODT, (u4){d.x, d.y, c.E, 0u});
        } else st16(c.r, t < 8 ? (unsigned)(cb * 128 + t * 16) : 0x80000000u, OFF_MOD, o);
    }
    BAR();
}
DI void mod_load(const Params& p, const Ctx& c, int cb, u4 (&wv)[9]) {
#pragma unroll
    for (int i = 0; i < 9; ++i) wv[i] = NT_W ? __builtin_nontemporal_load((const u4*)(p.wmod + (size_t)((c.tid >> 3) * 9 + i) * 3456 + cb * 64 + (c.tid & 7) * 8)) : *(const u4*)(p.wmod + (size_t)((c.tid >> 3) * 9 + i) * 3456 + cb * 64 + (c.tid & 7) * 8);
}
DI void prep_mod(const Params& p, Ctx& c, LAS char* lds, int j) {
    float sc[9];
    u4 wa[9], wb[9], wc[9];
    mod_load(p, c, j < 18 ? j : 18 + j, wa);
    bf16 cv[9];
#pragma unroll
    for (int i = 0; i < 9; ++i) cv[i] = p.cvec[(c.tid >> 3) * 9 + i];
    __builtin_amdgcn_sched_barrier(0);
    if (c.wave == 0) wait_until(c.n0 + EP_AT);
    const u4 epv = ep_ask(c);
    __builtin_amdgcn_sched_barrier(0);
#pragma unroll
    for (int i = 0; i < 9; ++i) { const float x = (float)cv[i]; sc[i] = rbf(x / (1.0f + __expf(-x))); }
    STMPM(0, 0, 3);
    if (c.wave == 0) { ep_take(c, epv); ep_get(c); }
    STMPM(0, 0, 4);
    mod_block(p, c, lds, j < 18 ? j : 18 + j, wa, sc);
    mod_load(p, c, j < 18 ? 18 + j : 24 + j, wb);
    mod_load(p, c, j < 6 ? 48 + j : j, wc);
    __builtin_amdgcn_sched_barrier(0);
    mod_block(p, c, lds, j < 18 ? 18 + j : 24 + j, wb, sc);
    if (j < 6) mod_block(p, c, lds, 48 + j, wc, sc);
    publish(c, FL_MOD, 1, j);
    if (c.wave == 0) wait_flags(c, FL_MOD, 1, 0, NPREP);
    BAR();
}
constexpr int NLN1 = 72;
DI void prep_ln(const Params& p, Ctx& c, LAS char* lds, int j) {
    const int lane = c.lane, w = c.wave;
    float xa[9], xb[9], xc[9], xd[9];
    row_load(p.x + (size_t)(j * 8 + w) * 576, lane, xa); row_load(p.x + (size_t)((j + NLN1) * 8 + w) * 576, lane, xb);
    row_load(p.x + (size_t)((j < 40 ? j + 3 * NLN1 : j) * 8 + w) * 576, lane, xd); row_load(p.x + (size_t)((j + 2 * NLN1) * 8 + w) * 576, lane, xc);
    float sa[9], ca[9];
    {
        LAS char* MS = lds + 24576;
        if (w == 0) {
            hold_back(PL_HOLD);
            ep_get(c);
            unsigned spins = 0, nv = 0;
            for (;;) {
                CFENCE();
                u4 q[5]; bool ok = true;
#pragma unroll
                for (int i = 0; i < 5; ++i) q[i] = ld16(c.r, (i < 4 || lane < 32) ? (unsigned)(i * 1024 + lane * 16) : 0x80000000u, OFF_MODT);
                const u2 ni = ld8(c.r, 0, OFF_INIT);
#pragma unroll
                for (int i = 0; i < 5; ++i) { if (i < 4 || lane < 32) { *(LAS u2*)(MS + i * 512 + lane * 8) = (u2){q[i].x, q[i].y}; ok = ok && q[i].z == c.E; } }
                nv = ni.y;
                if (__all(ok && ni.x == c.E)) break;
                __builtin_amdgcn_s_sleep(PL_SLEEP);
                if (spin_fail(c, spins)) break;
            }
            near_take(c, nv);
            STMPM(0, 0, 4);
        }
        BAR();
        unpack8(*(LAS u4*)(MS + lane * 16), sa); sa[8] = __uint_as_float((unsigned)*(LAS unsigned short*)(MS + (512 + lane) * 2) << 16);
        unpack8(*(LAS u4*)(MS + 1152 + lane * 16), ca); ca[8] = __uint_as_float((unsigned)*(LAS unsigned short*)(MS + 1152 + (512 + lane) * 2) << 16);
    }
    ln_mod(xa, ca, sa); ln_mod(xb, ca, sa);
    row_store(c, OFF_H1 + (j * 8 + w) * 1152, xa, lds + w * 128);
    row_store(c, OFF_H1 + ((j + NLN1) * 8 + w) * 1152, xb, lds + w * 128);
    DRAIN(); BAR();
    if (c.tid == 0) { set_flag(c, FL_H1, j >> 2, j & 3); set_flag(c, FL_H1, (j + NLN1) >> 2, (j + NLN1) & 3); }
    STMPW(0, j >> 2, 7);
    ln_mod(xc, ca, sa); ln_mod(xd, ca, sa);
    row_store(c, OFF_H1 + ((j + 2 * NLN1) * 8 + w) * 1152, xc, lds + w * 128);
    if (j < 40) row_store(c, OFF_H1 + ((j + 3 * NLN1) * 8 + w) * 1152, xd, lds + w * 128);
    DRAIN(); BAR();
    if (c.tid == 0) { set_flag(c, FL_H1, (j + 2 * NLN1) >> 2, (j + 2 * NLN1) & 3); if (j < 40) set_flag(c, FL_H1, (j + 3 * NLN1) >> 2, (j + 3 * NLN1) & 3); }
    STMPW(0, (j + 2 * NLN1) >> 2, 7);
}
DI void role_ln2(const Params& p, Ctx& c, LAS char* lds, int id, const Seq q) {
    const int lane = c.lane, w = c.wave;
    float ga[9], sb[9], cb[9];
    q.rest(c);
    near_setup(c);
    if (q.help) wait_flags(c, FL_MOD, 1, 0, NPREP);
    mod_row(c, 2, ga); mod_row(c, 3, sb); mod_row(c, 4, cb);
    const int rl = 8 * id + w;
    LAS unsigned* CTL = (LAS unsigned*)(lds + 2048);
    bool ready = false; int pending = -1;
    for (int s = q.first, it = 0; s < NSEG; s = q.nx(s), ++it) {
        const int s1 = q.nx(s);
        const int row = s * 32 + rl;
        float x[9], a[9];
        row_load(p.x + (size_t)row * 576, lane, x);
        STMPW(0, s, 0);
        if (!ready) { if (w == 0) wait_flags(c, FL_ATT, s, 0, 36); BAR(); }
        STMPM(0, s, 1);
        const u4 av = ld16(c.r, (lane >> 2) * 2048 + rl * 64 + (lane & 3) * 16, OFF_ATT + (s) * 18 * 2048);
        const unsigned a8 = __builtin_amdgcn_raw_buffer_load_b16(c.r, (16 + (lane >> 5)) * 2048 + rl * 64 + (lane & 31) * 2, OFF_ATT + (s) * 18 * 2048, 16);
        const bool on = LN2_DEFER && w == 0 && s1 < NSEG;
        const unsigned fn = ld4(c.r, on ? fl_off(c, FL_ATT, s1) + (lane < 36 ? lane : 0) * 4 : 0xc0000000u, 0);
        if (LN2_DEFER) {
            if (w == 0) { const bool qq = on && __all(fn == c.E); if (lane == 0) CTL[it & 1] = qq; }
            DRAIN(); BAR();
            if (pending >= 0 && c.tid == 0) set_flag(c, FL_H2, pending, id);
            pending = -1;
            ready = lds_u(CTL + (it & 1)) != 0u;
        }
        unpack8(av, a);
        a[8] = __uint_as_float(a8 << 16);
#pragma unroll
        for (int j = 0; j < 9; ++j) a[j] = ga[j] * a[j];
        rbf9(a);
#pragma unroll
        for (int j = 0; j < 9; ++j) x[j] = x[j] + a[j];
        rbf9(x);
        row_store(c, OFF_X1 + ((s) * 32 + rl) * 1152, x, lds + w * 128);
        ln_mod(x, cb, sb);
        row_store(c, OFF_H2 + ((s) * 32 + rl) * 1152, x, lds + w * 128);
        if (ready) pending = s; else publish(c, FL_H2, s, id);
        STMPW(0, s, 2);
    }
}
DI void role_ffn1(const Params& p, Ctx& c, LAS char* lds, int j, const Seq q) {
    const int lane = c.lane, w = c.wave;
    bf8 wg[18], wu[18];
    Panel<128, 576> pa_, pb_;
    pa_.load(p.Wg + j * 128, F_, c.tid); pb_.load(p.Wu + j * 128, F_, c.tid);
    pa_.store(lds, c.tid); __syncthreads();
#pragma unroll
    for (int ks = 0; ks < 18; ++ks) wg[ks] = frag_dense(lds, 272, ks * 32, w * 32, lane);
    __syncthreads();
    pb_.store(lds, c.tid); __syncthreads();
#pragma unroll
    for (int ks = 0; ks < 18; ++ks) wu[ks] = frag_dense(lds, 272, ks * 32, w * 32, lane);
    __syncthreads();
    LAS char* AI0 = lds + 2 * IMG_BYTES + 64;
    q.rest(c);
    near_setup(c);
    if (FFN_PRIO == 1 && w < 4) __builtin_amdgcn_s_setprio(2);
    if (FFN_PRIO == 2 && (w & 1)) __builtin_amdgcn_s_setprio(2);
    pipeline<FL_H2, 4, FL_ACT, 1>(c, lds, OFF_H2, j, q, [&](int s, int it, LAS char* IMG, auto probe, auto mid, auto early, auto tick) {
        f4 g[2] = {{0, 0, 0, 0}, {0, 0, 0, 0}}, u[2] = {{0, 0, 0, 0}, {0, 0, 0, 0}};
        ks_loop([&](int ks, bf8 (&b)[2]) { tick(ks); if (ks == E0_KS) early(0); if (ks == E1_KS) early(1); if (ks == E2_KS) early(2); if (ks == 16) probe(); b[0] = img_frag(IMG, 0, ks, lane); b[1] = img_frag(IMG, 1, ks, lane); },
                [&](int ks, bf8 (&b)[2]) {
#pragma unroll
                    for (int t = 0; t < 2; ++t) { g[t] = mfma(wg[ks], b[t], g[t]); u[t] = mfma(wu[ks], b[t], u[t]); } });
        STMPW(FW, s, 5);
        LAS char* AI = AI0 + (it & 1) * 8192;
        {
            float gg[8], uu[8], e[8];
#pragma unroll
            for (int q = 0; q < 4; ++q) {
                const unsigned a = pk2(g[q >> 1][2 * (q & 1)], g[q >> 1][2 * (q & 1) + 1]), b2 = pk2(u[q >> 1][2 * (q & 1)], u[q >> 1][2 * (q & 1) + 1]);
                gg[2 * q] = lo16(a); gg[2 * q + 1] = hi16(a); uu[2 * q] = lo16(b2); uu[2 * q + 1] = hi16(b2);
            }
#pragma unroll
            for (int r = 0; r < 8; ++r) e[r] = __builtin_amdgcn_exp2f(-gg[r] * LOG2E);
#pragma unroll
            for (int r = 0; r < 8; ++r) e[r] = gg[r] * __builtin_amdgcn_rcpf(1.0f + e[r]);
            unsigned o[4];
#pragma unroll
            for (int q = 0; q < 4; ++q) { const unsigned a = pk2(e[2 * q], e[2 * q + 1]); o[q] = pk2(lo16(a) * uu[2 * q], hi16(a) * uu[2 * q + 1]); }
#pragma unroll
            for (int t = 0; t < 2; ++t) *(LAS u2*)(AI + t * 4096 + (w >> 1) * 1024 + lane * 16 + (w & 1) * 8) = (u2){o[2 * t], o[2 * t + 1]};
        }
        STMPW(FW, s, 6);
        mid();
        STMPW(FW, s, 7);
        st16(c.r, c.tid * 16, OFF_ACT + ((s) * 36 + j) * 8192, *(LAS u4*)(AI + c.tid * 16));
    });
}

DI void role_ffn2(const Params& p, Ctx& c, LAS char* lds, int id, const Seq sq) {
    const int lane = c.lane, w = c.wave;
    const int kq = id / 9, nb = id % 9, ct = w & 3, kh = w >> 2;
    bf8 wd[18];
    Panel<64, 576> pp_[2];
#pragma unroll
    for (int ps = 0; ps < 2; ++ps) pp_[ps].load(p.Wd + (size_t)(kq * 1152 + ps * 576) * 576 + nb * 64, 576, c.tid);
#pragma unroll
    for (int ps = 0; ps < 2; ++ps) {
        pp_[ps].store(lds, c.tid); __syncthreads();
        if (kh == ps) {
#pragma unroll
            for (int ks = 0; ks < 18; ++ks) wd[ks] = frag_perm(lds, 144, ks * 32, ct * 32, lane);
        }
        __syncthreads();
    }
    LAS char* AI = lds;
    LAS char* RED = lds + 73728;
    LAS unsigned* CTL = (LAS unsigned*)(lds + 73728 + 8192);
    auto load = [&](int s, u4 (&v)[9]) {
#pragma unroll
        for (int m = 0; m < 9; ++m) v[m] = ld16(c.r, c.tid * 16, OFF_ACT + (s * 36 + kq * 9 + m) * 8192); };
    auto store = [&](const u4 (&v)[9]) {
#pragma unroll
        for (int m = 0; m < 9; ++m) *(LAS u4*)(AI + m * 8192 + c.tid * 16) = v[m]; };
    auto peek = [&](int s) { CFENCE(); return ld4(c.r, (lane < 9 ? lane : 0) * 4, fl_off(c, FL_ACT, s) + kq * 36); };
    LAS char* Y = lds + 73728 + 8192 + 64;
    near_setup(c);
    wait_flags(c, FL_MOD, 1, 0, NPREP);
    const u2 gb = ld8(c.r, (nb * 64 + ct * 16 + 4 * (lane >> 4)) * 2, OFF_MOD + 5 * 1152);
    if (w == 0) wait_flags(c, FL_ACT, sq.first, kq * 9, 9);
    BAR();
    { u4 v0[9]; load(sq.first, v0); store(v0); }
    if (w == 0) { const bool r = sq.nx(sq.first) < NSEG && check_flags(c, FL_ACT, sq.nx(sq.first), kq * 9, 9); if (lane == 0) CTL[0] = r; }
    BAR();
    bool rdy = lds_u(CTL) != 0;
    const int word = nb * 4 + kq;
    f4 own[2], keep[2];
    int owed = -1;
    auto finish = [&](int s) {
        const int n = c.tid & 255, row = n >> 3, ch = n & 7;
        u4 xv = {0, 0, 0, 0};
        if (kh == 1) xv = ld16(c.r, row * 1152 + ch * 16, OFF_X1 + s * 32 * 1152 + nb * 128);
        if (w == 0) {
            unsigned spins = 0;
            for (;;) {
                CFENCE();
                const unsigned v = ld4(c.r, (lane & 3) * 4, fl_off(c, FL_P2, s) + nb * 16);
                if (__all(v == c.E || (lane & 3) == kq)) break;
                __builtin_amdgcn_s_sleep(FLAG_SLEEP);
                if (spin_fail(c, spins)) break;
            }
            CFENCE();
        }
        BAR();
        if (kh == 0) {
            u4 o[2][3];
#pragma unroll
            for (int t = 0; t < 2; ++t)
#pragma unroll
                for (int k = 0; k < 3; ++k) o[t][k] = ld16(c.r, (ct * 2 + t) * 1024 + lane * 16, OFF_P2 + ((s * 9 + nb) * 4 + (kq ^ (k + 1))) * 8192);
#pragma unroll
            for (int t = 0; t < 2; ++t) {
                float y[4];
#pragma unroll
                for (int j = 0; j < 4; ++j) y[j] = rbf((keep[t][j] + __uint_as_float(o[t][0][j])) + (__uint_as_float(o[t][1][j]) + __uint_as_float(o[t][2][j])));
                *(LAS u2*)(Y + (16 * t + (lane & 15)) * 144 + ct * 32 + (lane >> 4) * 8) = (u2){pk2(lo16(gb.x) * y[0], hi16(gb.x) * y[1]), pk2(lo16(gb.y) * y[2], hi16(gb.y) * y[3])};
            }
        }
        BAR();
        if (kh == 1) {
            const u4 y = *(LAS u4*)(Y + row * 144 + ch * 16), x = xv;
            const u4 o = {pk2(lo16(x.x) + lo16(y.x), hi16(x.x) + hi16(y.x)), pk2(lo16(x.y) + lo16(y.y), hi16(x.y) + hi16(y.y)),
                          pk2(lo16(x.z) + lo16(y.z), hi16(x.z) + hi16(y.z)), pk2(lo16(x.w) + lo16(y.w), hi16(x.w) + hi16(y.w))};
            *(u4*)((char*)p.out + ((size_t)s * 32 + row) * 1152 + nb * 128 + ch * 16) = o;
        }
        STMPM(4, s, 5);
    };
    int pending = -1;
    if (c.tid == 0) CTL[4] = 0u;
    for (int s_ = sq.first, it_ = 0; s_ < NSEG; s_ = sq.nx(s_), ++it_) {
        const int s = __builtin_amdgcn_readfirstlane(s_), it = __builtin_amdgcn_readfirstlane(it_), s1 = sq.nx(s), s2 = sq.nx(s1);
        pending = __builtin_amdgcn_readfirstlane(pending); owed = __builtin_amdgcn_readfirstlane(owed); rdy = __builtin_amdgcn_readfirstlane((int)rdy) != 0;
        STMPM(0, s, 0);
        const bool last = s1 >= NSEG, red = ((s >= sq.from ? s >> 1 : s) & 3) == kq;
        if (last) rdy = false;
        u4 v[9];
        unsigned f = 0;
        if (rdy) load(s1, v);
        else if (w == 0 && !last) f = peek(s1);
        __builtin_amdgcn_sched_barrier(0);
        f4 d[2] = {{0, 0, 0, 0}, {0, 0, 0, 0}};
        ks_loop([&](int ks, bf8 (&b)[2]) { const int kk = kh * 18 + ks; LAS char* a = AI + (kk >> 2) * 8192 + (kk & 3) * 1024 + lane * 16; b[0] = asbf8(*(LAS u4*)a); b[1] = asbf8(*(LAS u4*)(a + 4096)); },
                [&](int ks, bf8 (&b)[2]) { d[0] = mfma(wd[ks], b[0], d[0]); d[1] = mfma(wd[ks], b[1], d[1]); });
        if (pending >= 0) {
            if (kh == 0) {
                if (rdy) vm_wait<9>(); else DRAIN();
                if (lane == 0 && __hip_atomic_fetch_add(CTL + 4, 1u, __ATOMIC_RELAXED, __HIP_MEMORY_SCOPE_WORKGROUP) == 3u) { CTL[4] = 0u; set_flag(c, FL_P2, pending, word); }
            }
            pending = -1;
        }
        if (w == 0 && !rdy) { const bool q = !last && __all(f == c.E); if (lane == 0) CTL[3] = q; }
        if (kh == 1) {
#pragma unroll
            for (int t = 0; t < 2; ++t) *(LAS f4*)(RED + (ct * 2 + t) * 1024 + lane * 16) = d[t];
        }
        BAR();
        bool have = rdy;
        if (!rdy && lds_u(CTL + 3) != 0) { load(s1, v); have = true; }
        if (kh == 0) {
#pragma unroll
            for (int t = 0; t < 2; ++t) {
                own[t] = d[t] + *(LAS f4*)(RED + (ct * 2 + t) * 1024 + lane * 16);
                st16(c.r, red ? 0x80000000u : (unsigned)((ct * 2 + t) * 1024 + lane * 16), red ? 0u : OFF_P2 + ((s * 9 + nb) * 4 + kq) * 8192, __builtin_bit_cast(u4, own[t]));
            }
        }
        if (red) { keep[0] = own[0]; keep[1] = own[1]; }
        STMPM(0, s, 2);
        if (have) {
            unsigned fv = 0;
            if (s2 < NSEG && w == 0) fv = peek(s2);
            store(v);
            if (w == 0) { const bool nr = s2 < NSEG && __all(fv == c.E); if (lane == 0) CTL[(it + 1) & 1] = nr; }
            BAR();
            if (red) owed = s;
            else {
                if (owed >= 0 || s >= FFN2_IMM) {
                    DRAIN(); BAR();
                    if (c.tid == 0) set_flag(c, FL_P2, s, word);
                    if (owed >= 0) { finish(owed); owed = -1; }
                } else pending = s;
            }
            STMPM(0, s, 3);
        } else {
            if (w == 0) { const bool n1 = !last && check_flags(c, FL_ACT, s1, kq * 9, 9); if (lane == 0) CTL[2] = n1; }
            if (red) {
                BAR();
                if (lds_u(CTL + 2) != 0) owed = s; else finish(s);
            } else {
                DRAIN(); BAR();
                if (c.tid == 0) set_flag(c, FL_P2, s, word);
                if (owed >= 0) { finish(owed); owed = -1; }
            }
            STMPM(0, s, 3);
            if (last) break;
            if (lds_u(CTL + 2) == 0) { if (w == 0) wait_flags(c, FL_ACT, s1, kq * 9, 9); BAR(); }
            u4 v2[9];
            load(s1, v2);
            unsigned fw = 0;
            if (s2 < NSEG && w == 0) fw = peek(s2);
            store(v2);
            if (w == 0) { const bool nr = s2 < NSEG && __all(fw == c.E); if (lane == 0) CTL[(it + 1) & 1] = nr; }
            BAR();
        }
        rdy = lds_u(CTL + ((it + 1) & 1)) != 0;
        STMPW(0, s, 4);
    }
}

__host__ __device__ inline void role_of(int b, int& role, int& id) {
    const int x = b & 7, k = b >> 3;
    if (x < 3) {
        const int n = k * 3 + x;
        if (n < 32) { role = 0; id = n; } else if (n < 64) { role = 1; id = n - 32; } else if (n < 82) { role = 2; id = n - 64; } else { role = 8; id = n - 82; }
    } else if (x == 3) { if (k < 9) { role = 5; id = k; } else if (k < 18) { role = 7; id = k - 9; } else { role = 3; id = k - 18; } }
    else if (x == 4) { if (k < 9) { role = 5; id = 9 + k; } else if (k < 18) { role = 7; id = k; } else { role = 3; id = 14 + (k - 18); } }
    else if (x == 5) { if (k < 18) { role = 7; id = 18 + k; } else if (k < 22) { role = 3; id = 28 + (k - 18); } else { role = 4; id = k - 22; } }
    else if (x == 6) { if (k < 18) { role = 6; id = k; } else { role = 4; id = 10 + (k - 18); } }
    else { if (k < 18) { role = 6; id = 18 + k; } else if (k < 26) { role = 4; id = 24 + (k - 18); } else if (k < 30) { role = 9; id = k - 26; } else { role = 10; id = k - 30; } }
}
__global__ void __launch_bounds__(512, 2) mega(Params p) {
    extern __shared__ __attribute__((aligned(16))) char smem[];
    LAS char* lds = (LAS char*)smem;
    Ctx c; c.ws = p.ws; c.r = __builtin_amdgcn_make_buffer_rsrc(p.ws, 0, WS_END, 0x00020000);
    { const unsigned long long a_ = (unsigned long long)p.ws; c.srd = (u4){(unsigned)a_, (unsigned)(a_ >> 32) & 0xffffu, WS_END, 0x00020000u}; }
    c.tid = threadIdx.x; c.lane = threadIdx.x & 63; c.wave = __builtin_amdgcn_readfirstlane(threadIdx.x >> 6);
    c.lds = lds; c.nr.init(p.ws); c.flip = 0; c.mb = 0; c.pb = 0; c.fa = 0; c.fb = 0; c.E = 0; c.blk = blockIdx.x;
    {
        LAS unsigned* nn = (LAS unsigned*)(lds + 161760);
        if (c.tid == 0) { const unsigned long long t_ = __builtin_amdgcn_s_memrealtime(); nn[0] = (unsigned)t_; nn[1] = (unsigned)(t_ >> 32); }
        if (TLB_WARM && c.wave == 7 && blockIdx.x < 8 * TLB_WARM) { dma16(c, lds + 157696, c.lane << 21, 4096 + 128 * (blockIdx.x & 31)); dma16(c, lds + 158720, (64u + c.lane) << 21, 4096 + 128 * (blockIdx.x & 31)); }
        __syncthreads();
        c.n0 = __builtin_amdgcn_readfirstlane(nn[0]); c.n1 = __builtin_amdgcn_readfirstlane(nn[1]);
    }
    const int b = blockIdx.x;
    int role, id;
    role_of(b, role, id);
    if (role <= 1 && T_CH) hold_back(T_CH);
    if (role == 0) role_sc(p, c, lds, id);
    else if (role == 1) role_vc(p, c, lds, id);
    else if (role == 2) role_rd(p, c, lds, id);
    else if (role == 3) role_qk(p, c, lds, id);
    else if (role == 4) role_vw(p, c, lds, id);
    else if (role == 5) prep_mod(p, c, lds, id);
    else if (role == 6) prep_ln(p, c, lds, id);
    else if (role == 7) prep_ln(p, c, lds, 36 + id);
    else if (role == 8) { if (id == 0) { if (c.wave == 0) near_measure(c); } else if (id == 1) { if (c.wave == 0) ep_master(c); } else if (c.wave == 0) ep_get(c); }
    else if (role == 9) prep_mod(p, c, lds, 18 + id);
    else if (role == 10) prep_mod(p, c, lds, 22 + id);
    {
        bool help = role == 3 || role == 4;
        int r2 = help ? -1 : role, i2 = id;
        bool h5, h6, h7, h9;
        if (CREW == 0) {
            if (role == 3) r2 = HELPR;
            if (role == 4) { if (id < 4) { r2 = HELPR; i2 = 32 + id; } else if (id < 22) { r2 = 5; i2 = id - 4; } else if (id < 26) { r2 = 9; i2 = id - 22; } }
            h5 = true; h9 = true; h6 = HELPR == 6; h7 = HELPR == 7;
        } else {
            if (role == 3) r2 = 7;
            if (role == 4) { if (id < 4) { r2 = 7; i2 = 32 + id; } else { r2 = 6; i2 = id - 4; } }
            if (role == 10) { help = true; r2 = 6; i2 = 28 + id; }
            if (role == 8 && id >= 2 && id < 12) { help = true; if (id < 8) { r2 = 6; i2 = 28 + id; } else { r2 = 9; i2 = id - 8; } }
            h5 = false; h9 = true; h6 = true; h7 = true;
        }
        if (help && S0 >= NSEG) r2 = -1;
        const bool two = r2 == 5 ? h5 : r2 == 6 ? h6 : r2 == 7 ? h7 : h9;
        const Seq q = {help ? S0 + 1 : 0, two ? S0 : NSEG, help, (role == 8 || role == 10) ? (unsigned)T_IDLE : 0u};
        if (help) {
            __syncthreads();
        }
        if (r2 == 5) role_pvw(p, c, lds, i2, q); else if (r2 == 6) role_ffn1(p, c, lds, i2, q); else if (r2 == 7) role_ffn2(p, c, lds, i2, q); else if (r2 == 9) role_ln2(p, c, lds, i2, q);
    }
    STMP_DUMP();
}

extern "C" void kernel_launch(void* const* d_in, const int* in_sizes, int n_in, void* d_out, int out_size, void* d_ws, size_t ws_size, hipStream_t stream) {
    (void)in_sizes; (void)n_in; (void)out_size; (void)ws_size;
    hipFuncSetAttribute((const void*)mega, hipFuncAttributeMaxDynamicSharedMemorySize, LDS_BYTES);
    Params p;
    p.x = (const bf16*)d_in[0]; p.mem0 = (const bf16*)d_in[1]; p.Wq = (const bf16*)d_in[2]; p.Wk = (const bf16*)d_in[3]; p.Wv = (const bf16*)d_in[4]; p.Wo = (const bf16*)d_in[5];
    p.Wg = (const bf16*)d_in[8]; p.Wu = (const bf16*)d_in[9]; p.Wd = (const bf16*)d_in[10]; p.cvec = (const bf16*)d_in[11]; p.wmod = (const bf16*)d_in[12]; p.out = (bf16*)d_out; p.ws = (char*)d_ws;
    mega<<<256, 512, LDS_BYTES, stream>>>(p);
}
```
